```python
import torch

def _softmax(x, axis=-1):
    return torch.softmax(x, dim=axis)
def _gelu(x):
    return torch.nn.functional.gelu(x, approximate="tanh")
def _silu(x):
    return torch.nn.functional.silu(x)
def _sigmoid(x):
    return torch.sigmoid(x)
def _layernorm(x, axis=-1, eps=1e-5):
    m = x.mean(dim=axis, keepdim=True)
    v = x.var(dim=axis, keepdim=True, unbiased=False)
    return (x - m) / torch.sqrt(v + eps)
def _scatter_add(tgt, idx, src, axis=0):
    return tgt.clone().index_add_(axis, idx, src)
def _segment_sum(src, seg, n):
    out = torch.zeros((n,) + src.shape[1:], dtype=src.dtype)
    return out.index_add_(0, seg, src)
def _pad(x, pw):
    flat = tuple(v for pair in reversed(pw) for v in pair)
    return torch.nn.functional.pad(x, flat)

kernel_name = 'recurrent_scan__softmax_full_mha__glu_gated_geglu__prompt'

def setup_inputs():

    _w = (lambda a: (lambda t: t.to(torch.bfloat16) if t.is_floating_point() else t)(torch.as_tensor(a)))
    def _A_setup():
        B, S, D = 1, 2048, 512
        return {
            "x": _w((lambda *s: torch.randn(*s, dtype=torch.bfloat16) * 0.05)(B, S, D)),
            "wqkv": _w((lambda *s: torch.randn(*s, dtype=torch.bfloat16) * 0.2)(D, 3 * D)),
            "wo": _w((lambda *s: torch.randn(*s, dtype=torch.bfloat16) * 0.2)(D, D)),
        }
    _A_in = _A_setup()

    def _B_setup():
        B, S, Dm, Df = 1, 2048, 512, 1792
        return {
            "x": _w((lambda *s: torch.randn(*s, dtype=torch.bfloat16) * 0.05)(B, S, Dm)),
            "w_gate": _w((lambda *s: torch.randn(*s, dtype=torch.bfloat16) * 0.1)(Dm, Df) * (Dm ** -0.5)),
            "w_up": _w((lambda *s: torch.randn(*s, dtype=torch.bfloat16) * 0.1)(Dm, Df) * (Dm ** -0.5)),
            "w_down": _w((lambda *s: torch.randn(*s, dtype=torch.bfloat16) * 0.1)(Df, Dm) * (Df ** -0.5)),
        }
    _B_in = _B_setup()

    return {
        "A_x": _A_in["x"],
        "A_wqkv": _A_in["wqkv"],
        "A_wo": _A_in["wo"],
        "B_x": _B_in["x"],
        "B_w_gate": _B_in["w_gate"],
        "B_w_up": _B_in["w_up"],
        "B_w_down": _B_in["w_down"],
    }

def reference(A_x, A_wqkv, A_wo, B_x, B_w_gate, B_w_up, B_w_down):
    def _A_ref(x, wqkv, wo):
        B, S, D = x.shape
        H = 32; Dh = D // H
        qkv = (lambda x, *d: x.permute(*d))((x @ wqkv).reshape(B, S, 3, H, Dh), 2, 0, 3, 1, 4)
        q, k, v = qkv[0], qkv[1], qkv[2]
        scale = Dh ** -0.5
        scores = torch.einsum("bhqd,bhkd->bhqk", q, k) * scale
        p = _softmax(scores, axis=-1)
        o = torch.einsum("bhqk,bhkd->bhqd", p, v)
        return (lambda x, *d: x.permute(*d))(o, 0, 2, 1, 3).reshape(B, S, D) @ wo

    def _B_ref(x, w_gate, w_up, w_down):
        g = torch.einsum("bsd,df->bsf", x, w_gate)
        u = torch.einsum("bsd,df->bsf", x, w_up)
        h = _gelu(g) * u
        return torch.einsum("bsf,fd->bsd", h, w_down)

    x = A_x
    xn = _layernorm(x, axis=-1)
    s = _A_ref(xn, A_wqkv, A_wo)
    g = _silu(_B_ref(xn, B_w_gate, B_w_up, B_w_down))
    return x + s * g
```

```cpp
#include <hip/hip_runtime.h>
#include <hip/hip_bf16.h>
#include <stdint.h>

typedef short bf16x8 __attribute__((ext_vector_type(8)));
typedef short s16x4 __attribute__((ext_vector_type(4)));
typedef short v4i16_t __attribute__((ext_vector_type(4)));
typedef float f32x16 __attribute__((ext_vector_type(16)));
typedef float f32x4 __attribute__((ext_vector_type(4)));
typedef float f32x2 __attribute__((ext_vector_type(2)));
typedef unsigned u32x4 __attribute__((ext_vector_type(4)));
typedef unsigned u32x2 __attribute__((ext_vector_type(2)));
typedef __bf16 bf2_t __attribute__((ext_vector_type(2)));
#define LDSP __attribute__((address_space(3)))
typedef LDSP const char* lds_cptr;
typedef LDSP char* lds_ptr;

constexpr int SEQ = 2048, DM = 512, DF = 1792, NH = 32;
constexpr size_t WS_Q = 0, WS_K = 2u << 20, WS_V = 4u << 20, WS_O = 6u << 20, WS_H = 8u << 20, WS_STATS = 16u << 20, WS_CS = (16u << 20) + 65536;
constexpr float QSCALE = 0.25f * 1.4426950408889634f;

#define MFMA32(a, b, c) __builtin_amdgcn_mfma_f32_32x32x16_bf16((a), (b), (c), 0, 0, 0)
#define SBAR() __builtin_amdgcn_sched_barrier(0)
#define PIN(x) asm volatile("" : "+v"(x))

__device__ __forceinline__ unsigned cvtpk(float lo, float hi) { return __builtin_bit_cast(unsigned, __builtin_convertvector(((f32x2){lo, hi}), bf2_t)); }
__device__ __forceinline__ void glds16(const void* gbase, unsigned voff, unsigned lds_base) {
    unsigned sv;
    asm volatile("s_mov_b32 %0, m0\n\ts_mov_b32 m0, %3\n\ts_nop 0\n\tglobal_load_lds_dwordx4 %1, %2\n\ts_mov_b32 m0, %0"
                 : "=&s"(sv) : "v"(voff), "s"(gbase), "s"(lds_base) : "memory");
}
__device__ __forceinline__ bf16x8 ldsr128(lds_cptr p) { return *(const LDSP bf16x8*)p; }
__device__ __forceinline__ s16x4 vtr(lds_cptr p) { return __builtin_bit_cast(s16x4, __builtin_amdgcn_ds_read_tr16_b64_v4i16((LDSP v4i16_t*)p)); }
__device__ __forceinline__ bf16x8 cat8(s16x4 a, s16x4 b) { return (bf16x8){a[0], a[1], a[2], a[3], b[0], b[1], b[2], b[3]}; }
__device__ __forceinline__ float dot2(unsigned a, unsigned b, float c) { return __builtin_amdgcn_fdot2_f32_bf16(__builtin_bit_cast(bf2_t, a), __builtin_bit_cast(bf2_t, b), c, false); }
__device__ __forceinline__ float swap_add(float v) {
    auto rr = __builtin_amdgcn_permlane32_swap(__float_as_uint(v), __float_as_uint(v), false, false);
    return __uint_as_float(rr[0]) + __uint_as_float(rr[1]);
}
__device__ __forceinline__ float swap_max(float v) {
    auto rr = __builtin_amdgcn_permlane32_swap(__float_as_uint(v), __float_as_uint(v), false, false);
    return __builtin_fmaxf(__uint_as_float(rr[0]), __uint_as_float(rr[1]));
}
#define WAITV(n) asm volatile("s_waitcnt vmcnt(" #n ")" ::: "memory")
#define BARRIER() asm volatile("s_waitcnt lgkmcnt(0)\n\ts_barrier" ::: "memory")
#define STAMP(kid, slot) do {} while (0)
__device__ __forceinline__ float bflo(unsigned u) { return __uint_as_float(u << 16); }
__device__ __forceinline__ float bfhi(unsigned u) { return __uint_as_float(u & 0xffff0000u); }

constexpr int Q_ST = 4, Q_AB = 128 * 64, Q_WB = 32 * 192, Q_SB = Q_AB + Q_WB;
constexpr int Q_LDS = Q_ST * Q_SB + 4096;
__global__ __launch_bounds__(256, 2) void qkv_kernel(const char* __restrict__ x, const char* __restrict__ wqkv, char* __restrict__ ws) {
    extern __shared__ __attribute__((aligned(16))) char lds[];
    const int tid = threadIdx.x, lane = tid & 63, r = lane & 31, hh = lane >> 5;
    const int w = __builtin_amdgcn_readfirstlane(tid >> 6);
    const int b = blockIdx.x, xcd = b & 7, bi = b >> 3, rb = 4 * (xcd & 3) + (bi & 3), hp = 8 * (xcd >> 2) + (bi >> 2);
    const int m0 = rb * 128;
    const unsigned lds0 = (unsigned)(uintptr_t)lds;
    STAMP(0, 0);
    unsigned aoff[2], woff[2];
#pragma unroll
    for (int i = 0; i < 2; ++i) { const int row = 16 * (2 * w + i) + (lane >> 2), c = (lane & 3) ^ ((row >> 2) & 3); aoff[i] = row * 1024 + c * 16; }
#pragma unroll
    for (int i = 0; i < 2; ++i) { const int p = 64 * (w + 4 * i) + lane, row = p / 12, ch = p % 12; woff[i] = row * 3072 + (ch >> 2) * 1024 + hp * 64 + (ch & 3) * 16; }
    const char* ag = x + (size_t)m0 * 1024;
    const char* wg = wqkv;
    auto issue = [&](int kt) {
        const unsigned sb = lds0 + (kt & (Q_ST - 1)) * Q_SB;
        const char* ga = ag + kt * 64; const char* gw = wg + (size_t)kt * 32 * 3072;
        glds16(ga, aoff[0], sb + (2 * w) * 1024); glds16(ga, aoff[1], sb + (2 * w + 1) * 1024);
        glds16(gw, woff[0], sb + Q_AB + w * 1024);
        if (w < 2) glds16(gw, woff[1], sb + Q_AB + (4 + w) * 1024);
    };
    issue(0); issue(1); issue(2);
    f32x16 accQ = {}, accK = {}, accV = {};
    float sx = 0.f, sxx = 0.f, cs0 = 0.f, cs1 = 0.f, cs2 = 0.f;
    const int arow = 32 * w + r;
    const unsigned a_rd0 = arow * 64 + ((0 + hh) ^ ((arow >> 2) & 3)) * 16, a_rd1 = arow * 64 + ((2 + hh) ^ ((arow >> 2) & 3)) * 16;
    const unsigned w_rd = Q_AB + (8 * hh + ((lane & 15) >> 2)) * 192 + (2 * ((lane >> 4) & 1) + ((lane & 3) >> 1)) * 16 + 8 * (lane & 1);
    for (int kt = 0; kt < 16; ++kt) {
        if (kt < 13) { if (w < 2) WAITV(8); else WAITV(6); } else WAITV(0);
        BARRIER();
        if (kt == 0) STAMP(0, 1);
        if (kt + 3 < 16) issue(kt + 3);
        lds_cptr sb = (lds_cptr)lds + (kt & (Q_ST - 1)) * Q_SB;
#pragma unroll
        for (int kk = 0; kk < 2; ++kk) {
            const bf16x8 xa = ldsr128(sb + (kk ? a_rd1 : a_rd0));
            bf16x8 wb[3];
#pragma unroll
            for (int t = 0; t < 3; ++t) wb[t] = cat8(vtr(sb + w_rd + kk * 16 * 192 + t * 64), vtr(sb + w_rd + kk * 16 * 192 + 4 * 192 + t * 64));
            accQ = MFMA32(wb[0], xa, accQ); accK = MFMA32(wb[1], xa, accK); accV = MFMA32(xa, wb[2], accV);
            const u32x4 xu = __builtin_bit_cast(u32x4, xa);
#pragma unroll
            for (int j = 0; j < 4; ++j) { sx = dot2(xu[j], 0x3f803f80u, sx); sxx = dot2(xu[j], xu[j], sxx); }
            if ((kt & 1) == (w >> 1) && kk == (w & 1)) {
                const u32x4 u0 = __builtin_bit_cast(u32x4, wb[0]), u1 = __builtin_bit_cast(u32x4, wb[1]), u2 = __builtin_bit_cast(u32x4, wb[2]);
#pragma unroll
                for (int j = 0; j < 4; ++j) { cs0 = dot2(u0[j], 0x3f803f80u, cs0); cs1 = dot2(u1[j], 0x3f803f80u, cs1); cs2 = dot2(u2[j], 0x3f803f80u, cs2); }
            }
        }
    }
    STAMP(0, 2);
    float* sc = (float*)(lds + Q_ST * Q_SB);
    cs0 = swap_add(cs0); cs1 = swap_add(cs1); cs2 = swap_add(cs2);
    if (hh == 0) { sc[w * 96 + r] = cs0; sc[w * 96 + 32 + r] = cs1; sc[w * 96 + 64 + r] = cs2; }
    sx = swap_add(sx); sxx = swap_add(sxx);
    const float mu = sx * (1.f / 512.f); const float var = fmaxf(sxx * (1.f / 512.f) - mu * mu, 0.f); const float rstd = __builtin_amdgcn_rsqf(var + 1e-5f);
    if (hh == 0) { sc[512 + 2 * arow] = mu; sc[512 + 2 * arow + 1] = rstd;
        if (hp == 0) { ((f32x2*)(ws + WS_STATS))[m0 + arow] = (f32x2){mu, rstd}; } }
    __syncthreads();
    if (tid < 96) sc[384 + tid] = sc[tid] + sc[96 + tid] + sc[192 + tid] + sc[288 + tid];
    __syncthreads();
    const int tile = rb * 4 + w;
    {
        const float qs = rstd * QSCALE;
#pragma unroll
        for (int which = 0; which < 2; ++which) {
            const f32x16& acc = which ? accK : accQ; const float scl = which ? rstd : qs;
            unsigned pk[8];
#pragma unroll
            for (int g = 0; g < 4; ++g) { const f32x4 c4 = *(const f32x4*)&sc[384 + which * 32 + 8 * g + 4 * hh];
                const float v0 = (acc[4 * g] - mu * c4[0]) * scl, v1 = (acc[4 * g + 1] - mu * c4[1]) * scl, v2 = (acc[4 * g + 2] - mu * c4[2]) * scl, v3 = (acc[4 * g + 3] - mu * c4[3]) * scl;
                pk[2 * g] = cvtpk(v0, v1); pk[2 * g + 1] = cvtpk(v2, v3); }
            char* dst = ws + (which ? WS_K : WS_Q);
#pragma unroll
            for (int hd = 0; hd < 2; ++hd) *(u32x4*)(dst + ((size_t)((2 * hp + hd) * 64 + tile) * 64 + lane) * 16) = (u32x4){pk[4 * hd], pk[4 * hd + 1], pk[4 * hd + 2], pk[4 * hd + 3]};
        }
    }
    {
        const float csv = sc[384 + 64 + r];
        unsigned pk[8];
#pragma unroll
        for (int g = 0; g < 4; ++g) { const f32x4 s0 = *(const f32x4*)&sc[512 + 2 * (32 * w + 8 * g + 4 * hh)], s1 = *(const f32x4*)&sc[512 + 2 * (32 * w + 8 * g + 4 * hh) + 4];
            const float v0 = (accV[4 * g] - s0[0] * csv) * s0[1], v1 = (accV[4 * g + 1] - s0[2] * csv) * s0[3], v2 = (accV[4 * g + 2] - s1[0] * csv) * s1[1], v3 = (accV[4 * g + 3] - s1[2] * csv) * s1[3];
            pk[2 * g] = cvtpk(v0, v1); pk[2 * g + 1] = cvtpk(v2, v3); }
        const int head = 2 * hp + (r >> 4), d = r & 15;
#pragma unroll
        for (int s = 0; s < 2; ++s) *(u32x4*)(ws + WS_V + ((((size_t)head * 128 + tile * 2 + s) * 2 + hh) * 16 + d) * 16) = (u32x4){pk[4 * s], pk[4 * s + 1], pk[4 * s + 2], pk[4 * s + 3]};
    }
    STAMP(0, 3);
}

#define REFB 88.f
constexpr float REF_BIAS = REFB;
template <int NQ>
__device__ __forceinline__ void attn_slow(const char* kf, const char* vf, const bf16x8* qf, f32x16* o, int lane) {
    const int r = lane & 31;
#pragma unroll
    for (int n = 0; n < NQ; ++n) {
        float m = -INFINITY; f32x16 oa = {};
        for (int t = 0; t < 64; ++t) {
            const bf16x8 ka = *(const bf16x8*)(kf + (size_t)t * 1024 + lane * 16);
            f32x16 s = MFMA32(ka, qf[n], (f32x16){});
            float tm = s[0];
#pragma unroll
            for (int i = 1; i < 16; ++i) tm = fmaxf(tm, s[i]);
            tm = swap_max(tm);
            const float mn = fmaxf(m, tm);
            const float al = __builtin_amdgcn_exp2f(m - mn);
            m = mn;
#pragma unroll
            for (int i = 0; i < 16; ++i) oa[i] *= al;
            unsigned pk[8];
#pragma unroll
            for (int i = 0; i < 8; ++i) pk[i] = cvtpk(__builtin_amdgcn_exp2f(s[2 * i] - mn), __builtin_amdgcn_exp2f(s[2 * i + 1] - mn));
#pragma unroll
            for (int s2 = 0; s2 < 2; ++s2) {
                u32x4 va;
                if (r < 16) va = *(const u32x4*)(vf + ((size_t)(2 * t + s2) * 2 + (lane >> 5)) * 256 + r * 16);
                else { const unsigned c = (r == 16 || r == 20) ? 0x3f803f80u : 0u; va = (u32x4){c, c, c, c}; }
                oa = MFMA32(__builtin_bit_cast(bf16x8, va), __builtin_bit_cast(bf16x8, ((u32x4){pk[4 * s2], pk[4 * s2 + 1], pk[4 * s2 + 2], pk[4 * s2 + 3]})), oa);
            }
        }
        o[n] = oa;
    }
}

template <int NQ>
__global__ __launch_bounds__(512 / NQ, 2) void attn_kernel(char* __restrict__ ws) {
    constexpr int NW = 8 / NQ;
    extern __shared__ __attribute__((aligned(16))) char lds[];
    const int tid = threadIdx.x, lane = tid & 63, r = lane & 31, hh = lane >> 5;
    const int w = __builtin_amdgcn_readfirstlane(tid >> 6);
    const int b = blockIdx.x, xcd = b & 7, bi = b >> 3, head = xcd * 4 + (bi & 3), qb = bi >> 2;
    const unsigned lds0 = (unsigned)(uintptr_t)lds;
    const char* kf = ws + WS_K + (size_t)head * 65536; const char* vf = ws + WS_V + (size_t)head * 65536;
    STAMP(1, 0);
    for (int i = w; i < 64; i += NW) { glds16(kf + i * 1024, lane * 16, lds0 + i * 1024); glds16(vf + i * 1024, lane * 16, lds0 + 65536 + i * 1024); }
    for (int i = tid; i < 256; i += 64 * NW) { const int row = i & 15; const unsigned c = (row == 0 || row == 4) ? 0x3f803f80u : 0u; *(u32x4*)(lds + 131072 + i * 16) = (u32x4){c, c, c, c}; }
    bf16x8 qf[NQ];
#pragma unroll
    for (int n = 0; n < NQ; ++n) qf[n] = *(const bf16x8*)(ws + WS_Q + ((size_t)(head * 64 + qb * 8 + w * NQ + n) * 64 + lane) * 16);
    WAITV(0); BARRIER();
    STAMP(1, 1);
    const unsigned kaddr = lane * 16;
    unsigned vaddr = (r < 16) ? (65536 + hh * 256 + r * 16) : (131072 + hh * 256 + (r - 16) * 16);
    const unsigned vstep = (r < 16) ? 4096u : 0u;
    f32x16 o[NQ], cneg[NQ], sA[NQ], sB[NQ];
    {
        const bf16x8 k0 = ldsr128((lds_cptr)lds + kaddr), k1 = ldsr128((lds_cptr)lds + kaddr + 1024);
#pragma unroll
        for (int n = 0; n < NQ; ++n) {
            const f32x16 s0 = MFMA32(k0, qf[n], (f32x16){}), s1 = MFMA32(k1, qf[n], (f32x16){});
            float tm = fmaxf(s0[0], s1[0]);
#pragma unroll
            for (int i = 1; i < 16; ++i) tm = fmaxf(tm, fmaxf(s0[i], s1[i]));
            tm = swap_max(tm);
            const float c = -(tm + REF_BIAS);
#pragma unroll
            for (int i = 0; i < 16; ++i) { cneg[n][i] = c; o[n][i] = 0.f; }
            PIN(cneg[n]);
#pragma unroll
            for (int i = 0; i < 16; ++i) sA[n][i] = s0[i] + c;
        }
    }
    lds_cptr kp = (lds_cptr)lds + kaddr;
    lds_cptr vp = (lds_cptr)lds + vaddr;
    bf16x8 kfr = ldsr128(kp + 1024);
    bf16x8 v0f = ldsr128(vp), v1f;
    { u32x4 z = {0u, 0u, 0u, 0u}; v1f = __builtin_bit_cast(bf16x8, z); }
    u32x4 pk0[NQ], pk1[NQ];
#pragma unroll
    for (int n = 0; n < NQ; ++n) { pk0[n] = (u32x4){0u, 0u, 0u, 0u}; pk1[n] = (u32x4){0u, 0u, 0u, 0u}; }
#define EXP2(x) __builtin_amdgcn_exp2f(x)
#define EHALF(dst, src, o8) do { float e_[8]; _Pragma("unroll") for (int i_ = 0; i_ < 8; ++i_) e_[i_] = EXP2(src[o8 + i_]); PIN(e_[0]); PIN(e_[1]); PIN(e_[2]); PIN(e_[3]); PIN(e_[4]); PIN(e_[5]); PIN(e_[6]); PIN(e_[7]); \
        etmp[0] = e_[0]; etmp[1] = e_[1]; etmp[2] = e_[2]; etmp[3] = e_[3]; etmp[4] = e_[4]; etmp[5] = e_[5]; etmp[6] = e_[6]; etmp[7] = e_[7]; } while (0)
#define CHALF(dst) do { dst = (u32x4){cvtpk(etmp[0], etmp[1]), cvtpk(etmp[2], etmp[3]), cvtpk(etmp[4], etmp[5]), cvtpk(etmp[6], etmp[7])}; PIN(dst); } while (0)
#define BF(x) __builtin_bit_cast(bf16x8, x)
#define STEP(cur, nxt, tt) do { \
        float etmp[8]; \
        _Pragma("unroll") for (int n = 0; n < NQ; ++n) { nxt[n] = MFMA32(kfr, qf[n], cneg[n]); PIN(nxt[n]); } \
        SBAR(); \
        kfr = ldsr128(kp + ((tt) + 2) * 1024); \
        SBAR(); \
        _Pragma("unroll") for (int n = 0; n < NQ; ++n) { \
            EHALF(pk0[n], cur[n], 0); SBAR(); \
            o[n] = MFMA32(v1f, BF(pk1[n]), o[n]); PIN(o[n]); SBAR(); \
            if (n == NQ - 1) { v1f = ldsr128(vp + (tt) * 1024 + 512); SBAR(); } \
            CHALF(pk0[n]); SBAR(); \
        } \
        _Pragma("unroll") for (int n = 0; n < NQ; ++n) { \
            EHALF(pk1[n], cur[n], 8); SBAR(); \
            o[n] = MFMA32(v0f, BF(pk0[n]), o[n]); PIN(o[n]); SBAR(); \
            if (n == NQ - 1) { v0f = ldsr128(vp + ((tt) + 1) * 1024 + ((tt) == 3 ? vnext : 0)); SBAR(); } \
            CHALF(pk1[n]); SBAR(); \
        } \
    } while (0)
    const int vnext = (r < 16) ? 0 : -4096;
    for (int t4 = 0; t4 < 64; t4 += 4) {
        STEP(sA, sB, 0); STEP(sB, sA, 1); STEP(sA, sB, 2); STEP(sB, sA, 3);
        kp += 4096; vp += vstep;
    }
#pragma unroll
    for (int n = 0; n < NQ; ++n) o[n] = MFMA32(v1f, BF(pk1[n]), o[n]);
    STAMP(1, 2);
    bool bad = false;
#pragma unroll
    for (int n = 0; n < NQ; ++n) {
        bad |= !(o[n][8] < 1.0e37f) || !(o[n][8] > 0.f);
#pragma unroll
        for (int i = 0; i < 8; ++i) bad |= !(fabsf(o[n][i]) < 3.0e38f);
    }
    if (__any(bad)) attn_slow<NQ>(kf, vf, qf, o, lane);
#pragma unroll
    for (int n = 0; n < NQ; ++n) {
        const float rl = 1.0f / o[n][8];
        const u32x4 pk = {cvtpk(o[n][0] * rl, o[n][1] * rl), cvtpk(o[n][2] * rl, o[n][3] * rl), cvtpk(o[n][4] * rl, o[n][5] * rl), cvtpk(o[n][6] * rl, o[n][7] * rl)};
        const int row = qb * 256 + (w * NQ + n) * 32 + r;
        *(u32x4*)(ws + WS_O + (size_t)row * 1024 + head * 32 + hh * 16) = pk;
    }
    STAMP(1, 3);
}

constexpr int G_ST = 3, G_AB = 256 * 64, G_WB = 32 * 256, G_SB = G_AB + G_WB;
constexpr int G_LDS = G_ST * G_SB + 2048;
__global__ __launch_bounds__(256, 2) void gateup_kernel(const char* __restrict__ x, const char* __restrict__ wgt, const char* __restrict__ wup, char* __restrict__ ws) {
    extern __shared__ __attribute__((aligned(16))) char lds[];
    const int tid = threadIdx.x, lane = tid & 63, r = lane & 31, hh = lane >> 5;
    const int w = __builtin_amdgcn_readfirstlane(tid >> 6), wm = w >> 1, wn = w & 1;
    const int b = blockIdx.x, rb = b & 7, cb = b >> 3;
    const int m0 = rb * 256, f0 = cb * 64;
    const unsigned lds0 = (unsigned)(uintptr_t)lds;
    STAMP(2, 0);
    unsigned aoff[4];
#pragma unroll
    for (int i = 0; i < 4; ++i) { const int row = 16 * (4 * w + i) + (lane >> 2), c = (lane & 3) ^ ((row >> 2) & 3); aoff[i] = row * 1024 + c * 16; }
    unsigned woff;
    { const int row = 8 * w + (lane >> 3), ch = (lane & 7) ^ (((row >> 1) & 1) << 2); woff = row * (DF * 2) + f0 * 2 + ch * 16; }
    const char* ag = x + (size_t)m0 * 1024;
    auto issue = [&](int kt) {
        const unsigned sb = lds0 + (kt % G_ST) * G_SB;
        const char* ga = ag + kt * 64; const size_t go = (size_t)kt * 32 * (DF * 2);
#pragma unroll
        for (int i = 0; i < 4; ++i) glds16(ga, aoff[i], sb + (4 * w + i) * 1024);
        glds16(wgt + go, woff, sb + G_AB + w * 1024); glds16(wup + go, woff, sb + G_AB + 4096 + w * 1024);
    };
    issue(0); issue(1);
    f32x16 acc[4][2] = {};
    float csg = 0.f, csu = 0.f;
    unsigned a_rd[2];
    { const int arow = 128 * wm + r; a_rd[0] = arow * 64 + ((0 + hh) ^ ((arow >> 2) & 3)) * 16; a_rd[1] = arow * 64 + ((2 + hh) ^ ((arow >> 2) & 3)) * 16; }
    const int q4 = (lane & 15) >> 2;
    const unsigned chg = (4 * wn + 2 * ((lane >> 4) & 1) + ((lane & 3) >> 1)) ^ (((q4 >> 1) & 1) << 2);
    const unsigned w_rdg = G_AB + (8 * hh + q4) * 128 + chg * 16 + 8 * (lane & 1), w_rdu = w_rdg + 4096;
    int slot = 0;
    for (int kt = 0; kt < 16; ++kt) {
        if (kt < 15) WAITV(6); else WAITV(0);
        BARRIER();
        if (kt == 0) STAMP(2, 1);
        if (kt + 2 < 16) issue(kt + 2);
        lds_cptr sb = (lds_cptr)lds + slot * G_SB; slot = slot == G_ST - 1 ? 0 : slot + 1;
#pragma unroll
        for (int kk = 0; kk < 2; ++kk) {
            const bf16x8 gb = cat8(vtr(sb + w_rdg + kk * 2048), vtr(sb + w_rdg + kk * 2048 + 512));
            const bf16x8 ub = cat8(vtr(sb + w_rdu + kk * 2048), vtr(sb + w_rdu + kk * 2048 + 512));
#pragma unroll
            for (int i = 0; i < 4; ++i) {
                const bf16x8 xa = ldsr128(sb + a_rd[kk] + i * 2048);
                acc[i][0] = MFMA32(gb, xa, acc[i][0]); acc[i][1] = MFMA32(ub, xa, acc[i][1]);
            }
            if (wm == 0) {
                const u32x4 g4 = __builtin_bit_cast(u32x4, gb), u4 = __builtin_bit_cast(u32x4, ub);
#pragma unroll
                for (int j = 0; j < 4; ++j) { csg = dot2(g4[j], 0x3f803f80u, csg); csu = dot2(u4[j], 0x3f803f80u, csu); }
            }
        }
    }
    STAMP(2, 2);
    float* sc = (float*)(lds + G_ST * G_SB);
    csg = swap_add(csg); csu = swap_add(csu);
    if (wm == 0 && hh == 0) { sc[32 * wn + r] = csg; sc[64 + 32 * wn + r] = csu; }
    __syncthreads();
    f32x4 cg[4], cu[4];
#pragma unroll
    for (int g = 0; g < 4; ++g) { cg[g] = *(const f32x4*)&sc[32 * wn + 8 * g + 4 * hh]; cu[g] = *(const f32x4*)&sc[64 + 32 * wn + 8 * g + 4 * hh]; }
    constexpr float GA = -2.f * 0.7978845608028654f * 1.4426950408889634f, GB = GA * 0.044715f;
#pragma unroll
    for (int i = 0; i < 4; ++i) {
        const int row = m0 + 128 * wm + 32 * i + r;
        const f32x2 st = ((const f32x2*)(ws + WS_STATS))[row];
        const float mu = st[0], rstd = st[1];
        unsigned pk[8];
#pragma unroll
        for (int g = 0; g < 4; ++g) {
            float hv[4];
#pragma unroll
            for (int j = 0; j < 4; ++j) {
                const float gv = (acc[i][0][4 * g + j] - mu * cg[g][j]) * rstd, uv = (acc[i][1][4 * g + j] - mu * cu[g][j]) * rstd;
                const float z = gv * (GA + GB * gv * gv);
                hv[j] = gv * uv * __builtin_amdgcn_rcpf(1.f + __builtin_amdgcn_exp2f(z));
            }
            pk[2 * g] = cvtpk(hv[0], hv[1]); pk[2 * g + 1] = cvtpk(hv[2], hv[3]);
        }
        char* dst = ws + WS_H + (size_t)row * (DF * 2) + (f0 + 32 * wn) * 2 + hh * 16;
        *(u32x4*)dst = (u32x4){pk[0], pk[1], pk[2], pk[3]}; *(u32x4*)(dst + 32) = (u32x4){pk[4], pk[5], pk[6], pk[7]};
    }
    STAMP(2, 3);
}

constexpr int D_ST = 4, D_AB = 64 * 128, D_WB = 64 * 128, D_SB = D_AB + D_WB;
constexpr int D_LDS = D_ST * D_SB;
__global__ __launch_bounds__(256, 2) void down_kernel(const char* __restrict__ x, const char* __restrict__ wdn, const char* __restrict__ wo, char* __restrict__ ws, char* __restrict__ out) {
    extern __shared__ __attribute__((aligned(16))) char lds[];
    const int tid = threadIdx.x, lane = tid & 63, r = lane & 31, hh = lane >> 5;
    const int w = __builtin_amdgcn_readfirstlane(tid >> 6), wm = w >> 1, wn = w & 1;
    const int b = blockIdx.x, xcd = b & 7, bi = b >> 3, rb = xcd * 4 + (bi & 3), cb = bi >> 2;
    const int m0 = rb * 64, n0 = cb * 64;
    const unsigned lds0 = (unsigned)(uintptr_t)lds;
    STAMP(3, 0);
    unsigned aoffh[2], aoffo[2], woff[2];
#pragma unroll
    for (int i = 0; i < 2; ++i) { const int row = 8 * (2 * w + i) + (lane >> 3), c = (lane & 7) ^ ((row >> 1) & 7);
        aoffh[i] = row * (DF * 2) + c * 16; aoffo[i] = row * 1024 + c * 16;
        const int ch = (lane & 7) ^ (((row >> 1) & 1) << 2); woff[i] = row * 1024 + n0 * 2 + ch * 16; }
    const char* hg = ws + WS_H + (size_t)m0 * (DF * 2); const char* og = ws + WS_O + (size_t)m0 * 1024;
    constexpr int NT = 36;
    auto issue = [&](int kt) {
        const unsigned sb = lds0 + (kt & (D_ST - 1)) * D_SB;
        if (kt < 28) {
            const char* ga = hg + kt * 128; const char* gw = wdn + (size_t)kt * 64 * 1024;
            glds16(ga, aoffh[0], sb + (2 * w) * 1024); glds16(ga, aoffh[1], sb + (2 * w + 1) * 1024);
            glds16(gw, woff[0], sb + D_AB + (2 * w) * 1024); glds16(gw, woff[1], sb + D_AB + (2 * w + 1) * 1024);
        } else {
            const char* ga = og + (kt - 28) * 128; const char* gw = wo + (size_t)(kt - 28) * 64 * 1024;
            glds16(ga, aoffo[0], sb + (2 * w) * 1024); glds16(ga, aoffo[1], sb + (2 * w + 1) * 1024);
            glds16(gw, woff[0], sb + D_AB + (2 * w) * 1024); glds16(gw, woff[1], sb + D_AB + (2 * w + 1) * 1024);
        }
    };
    issue(0); issue(1); issue(2);
    f32x16 accY = {}, accS = {};
    const int arow = 32 * wm + r;
    unsigned a_rd[4];
#pragma unroll
    for (int kk = 0; kk < 4; ++kk) a_rd[kk] = arow * 128 + ((2 * kk + hh) ^ ((arow >> 1) & 7)) * 16;
    const int q4 = (lane & 15) >> 2;
    const unsigned chw = (4 * wn + 2 * ((lane >> 4) & 1) + ((lane & 3) >> 1)) ^ (((q4 >> 1) & 1) << 2);
    const unsigned w_rd = D_AB + (4 * hh + q4) * 128 + chw * 16 + 8 * (lane & 1);
    for (int kt = 0; kt < NT; ++kt) {
        if (kt < NT - 3) WAITV(8); else WAITV(0);
        BARRIER();
        if (kt == 0) STAMP(3, 1);
        if (kt + 3 < NT) issue(kt + 3);
        lds_cptr sb = (lds_cptr)lds + (kt & (D_ST - 1)) * D_SB;
        if (kt < 28) {
#pragma unroll
            for (int kk = 0; kk < 4; ++kk) { const bf16x8 wb = cat8(vtr(sb + w_rd + kk * 2048), vtr(sb + w_rd + kk * 2048 + 1024)); const bf16x8 xa = ldsr128(sb + a_rd[kk]); accY = MFMA32(wb, xa, accY); }
        } else {
#pragma unroll
            for (int kk = 0; kk < 4; ++kk) { const bf16x8 wb = cat8(vtr(sb + w_rd + kk * 2048), vtr(sb + w_rd + kk * 2048 + 1024)); const bf16x8 xa = ldsr128(sb + a_rd[kk]); accS = MFMA32(wb, xa, accS); }
        }
    }
    STAMP(3, 2);
    const int row = m0 + 32 * wm + r;
    const char* xr = x + (size_t)row * 1024 + (n0 + 32 * wn + 4 * hh) * 2;
    char* orow = out + (size_t)row * 1024 + (n0 + 32 * wn + 4 * hh) * 2;
#pragma unroll
    for (int g = 0; g < 4; ++g) {
        const u32x2 xv = *(const u32x2*)(xr + g * 16);
        float res[4];
#pragma unroll
        for (int j = 0; j < 4; ++j) {
            const float yv = accY[4 * g + j], sv = accS[4 * g + j];
            const float gate = yv * __builtin_amdgcn_rcpf(1.f + __builtin_amdgcn_exp2f(-1.4426950408889634f * yv));
            const float xx = (j & 1) ? bfhi(xv[j >> 1]) : bflo(xv[j >> 1]);
            res[j] = xx + sv * gate;
        }
        *(u32x2*)(orow + g * 16) = (u32x2){cvtpk(res[0], res[1]), cvtpk(res[2], res[3])};
    }
    STAMP(3, 3);
}

#define ATTN_NQ 1
extern "C" void kernel_launch(void* const* d_in, const int* in_sizes, int n_in, void* d_out, int out_size, void* d_ws, size_t ws_size, hipStream_t stream) {
    const char* x = (const char*)d_in[0]; const char* wqkv = (const char*)d_in[1]; const char* wo = (const char*)d_in[2];
    const char* wg = (const char*)d_in[4]; const char* wu = (const char*)d_in[5]; const char* wd = (const char*)d_in[6];
    char* ws = (char*)d_ws;
    constexpr int A_LDS = 131072 + 4096;
    (void)hipFuncSetAttribute((const void*)attn_kernel<ATTN_NQ>, hipFuncAttributeMaxDynamicSharedMemorySize, A_LDS);
    (void)hipFuncSetAttribute((const void*)gateup_kernel, hipFuncAttributeMaxDynamicSharedMemorySize, G_LDS);
    (void)hipFuncSetAttribute((const void*)down_kernel, hipFuncAttributeMaxDynamicSharedMemorySize, D_LDS);
    (void)hipFuncSetAttribute((const void*)qkv_kernel, hipFuncAttributeMaxDynamicSharedMemorySize, Q_LDS);
    qkv_kernel<<<256, 256, Q_LDS, stream>>>(x, wqkv, ws);
    attn_kernel<ATTN_NQ><<<256, 512 / ATTN_NQ, A_LDS, stream>>>(ws);
    gateup_kernel<<<224, 256, G_LDS, stream>>>(x, wg, wu, ws);
    down_kernel<<<256, 256, D_LDS, stream>>>(x, wd, wo, ws, (char*)d_out);
}
```

```cpp
#define NO_PF
#include <hip/hip_runtime.h>
#include <hip/hip_bf16.h>
#include <stdint.h>

typedef short bf16x8 __attribute__((ext_vector_type(8)));
typedef short s16x4 __attribute__((ext_vector_type(4)));
typedef short v4i16_t __attribute__((ext_vector_type(4)));
typedef float f32x16 __attribute__((ext_vector_type(16)));
typedef float f32x4 __attribute__((ext_vector_type(4)));
typedef float f32x2 __attribute__((ext_vector_type(2)));
typedef unsigned u32x4 __attribute__((ext_vector_type(4)));
typedef unsigned u32x2 __attribute__((ext_vector_type(2)));
typedef __bf16 bf2_t __attribute__((ext_vector_type(2)));
typedef unsigned long long u64;
#define LDSP __attribute__((address_space(3)))
#define GLBP __attribute__((address_space(1)))
typedef LDSP const char* lds_cptr;
typedef LDSP char* lds_ptr;

constexpr int SEQ = 2048, DM = 512, DF = 1792, NH = 32;
constexpr size_t WS_Q = 0, WS_K = 2u << 20, WS_V = 4u << 20, WS_O = 6u << 20, WS_H = 8u << 20, WS_STATS = 16u << 20, WS_FLAGS = (16u << 20) + 131072;
constexpr int FL_GEN = 0, FL_START = 16, FL_Q = 16 + 512, FL_H = FL_Q + 256, FL_O = FL_H + 512, FL_S = FL_O + 256, FL_END = FL_S + 256;
constexpr size_t WS_GST = (16u << 20), WS_GCS = (16u << 20) + 32768;
constexpr size_t WS_XN = 18u << 20;
constexpr float QSCALE = 0.25f * 1.4426950408889634f;
constexpr int LDS_BYTES = 81920;

#define MFMA32(a, b, c) __builtin_amdgcn_mfma_f32_32x32x16_bf16((a), (b), (c), 0, 0, 0)
#define SBAR() __builtin_amdgcn_sched_barrier(0)
#define PIN(x) asm volatile("" : "+v"(x))

__device__ __forceinline__ unsigned cvtpk(float lo, float hi) { return __builtin_bit_cast(unsigned, __builtin_convertvector(((f32x2){lo, hi}), bf2_t)); }
__device__ __forceinline__ unsigned uni(unsigned v) { return (unsigned)__builtin_amdgcn_readfirstlane((int)v); }
template <class T> __device__ __forceinline__ T* uni(T* p) { const u64 v = (u64)p; return (T*)(((u64)uni((unsigned)(v >> 32)) << 32) | uni((unsigned)v)); }
#define GLDS_ASM(SC) \
    if constexpr (NP == 1) asm volatile("s_mov_b32 m0, %2\n\ts_nop 0\n\tglobal_load_lds_dwordx4 %0, %1" SC :: "v"(v0), "s"(gbase), "s"(lds_base) : "memory"); \
    else if constexpr (NP == 2) asm volatile("s_mov_b32 m0, %3\n\ts_nop 0\n\tglobal_load_lds_dwordx4 %0, %2" SC "\n\tglobal_load_lds_dwordx4 %1, %2 offset:1024" SC :: "v"(v0), "v"(v1), "s"(gbase), "s"(lds_base) : "memory"); \
    else if constexpr (NP == 3) asm volatile("s_mov_b32 m0, %4\n\ts_nop 0\n\tglobal_load_lds_dwordx4 %0, %3" SC "\n\tglobal_load_lds_dwordx4 %1, %3 offset:1024" SC "\n\tglobal_load_lds_dwordx4 %2, %3 offset:2048" SC :: "v"(v0), "v"(v1), "v"(v2), "s"(gbase), "s"(lds_base) : "memory"); \
    else asm volatile("s_mov_b32 m0, %5\n\ts_nop 0\n\tglobal_load_lds_dwordx4 %0, %4" SC "\n\tglobal_load_lds_dwordx4 %1, %4 offset:1024" SC "\n\tglobal_load_lds_dwordx4 %2, %4 offset:2048" SC "\n\tglobal_load_lds_dwordx4 %3, %4 offset:3072" SC :: "v"(v0), "v"(v1), "v"(v2), "v"(v3), "s"(gbase), "s"(lds_base) : "memory");
template <bool SC1, int NP>
__device__ __forceinline__ void gldsN(const void* gbase, unsigned lds_base, unsigned v0, unsigned v1 = 0, unsigned v2 = 0, unsigned v3 = 0) {
    if constexpr (SC1) { GLDS_ASM(" sc1") } else { GLDS_ASM("") }
}
template <bool SC1>
__device__ __forceinline__ void glds16(const void* gbase, unsigned voff, unsigned lds_base) { gldsN<SC1, 1>(gbase, lds_base, voff); }
__device__ __forceinline__ void st16_sc1(void* gbase, unsigned voff, u32x4 v) { asm volatile("s_nop 4\n\tglobal_store_dwordx4 %0, %1, %2 sc1\n\ts_nop 1" :: "v"(voff), "v"(v), "s"(uni(gbase)) : "memory"); }
__device__ __forceinline__ void st8_sc1(void* gbase, unsigned voff, f32x2 v) { asm volatile("s_nop 4\n\tglobal_store_dwordx2 %0, %1, %2 sc1\n\ts_nop 1" :: "v"(voff), "v"(v), "s"(uni(gbase)) : "memory"); }
__device__ __forceinline__ bf16x8 ldsr128(lds_cptr p) { return *(const LDSP bf16x8*)p; }
__device__ __forceinline__ s16x4 vtr(lds_cptr p) { return __builtin_bit_cast(s16x4, __builtin_amdgcn_ds_read_tr16_b64_v4i16((LDSP v4i16_t*)p)); }
__device__ __forceinline__ bf16x8 cat8(s16x4 a, s16x4 b) { return (bf16x8){a[0], a[1], a[2], a[3], b[0], b[1], b[2], b[3]}; }
__device__ __forceinline__ float dot2(unsigned a, unsigned b, float c) { return __builtin_amdgcn_fdot2_f32_bf16(__builtin_bit_cast(bf2_t, a), __builtin_bit_cast(bf2_t, b), c, false); }
__device__ __forceinline__ float swap_add(float v) {
    auto rr = __builtin_amdgcn_permlane32_swap(__float_as_uint(v), __float_as_uint(v), false, false);
    return __uint_as_float(rr[0]) + __uint_as_float(rr[1]);
}
__device__ __forceinline__ float swap_max(float v) {
    auto rr = __builtin_amdgcn_permlane32_swap(__float_as_uint(v), __float_as_uint(v), false, false);
    return __builtin_fmaxf(__uint_as_float(rr[0]), __uint_as_float(rr[1]));
}
template <int CTRL> __device__ __forceinline__ float dpp_add(float v) { return v + __int_as_float(__builtin_amdgcn_update_dpp(0, __float_as_int(v), CTRL, 0xf, 0xf, true)); }
__device__ __forceinline__ float swap16_add(float v) {
    auto rr = __builtin_amdgcn_permlane16_swap(__float_as_uint(v), __float_as_uint(v), false, false);
    return __uint_as_float(rr[0]) + __uint_as_float(rr[1]);
}
__device__ __forceinline__ float wave_sum(float v) {
    v = dpp_add<0xB1>(v); v = dpp_add<0x4E>(v); v = dpp_add<0x141>(v); v = dpp_add<0x140>(v);
    return swap_add(swap16_add(v));
}
#define WAITV(n) asm volatile("s_waitcnt vmcnt(" #n ")" ::: "memory")
#define BARRIER() asm volatile("s_waitcnt lgkmcnt(0)\n\ts_barrier" ::: "memory")
__device__ __forceinline__ float bflo(unsigned u) { return __uint_as_float(u << 16); }
__device__ __forceinline__ float bfhi(unsigned u) { return __uint_as_float(u & 0xffff0000u); }

#define STAMP(slot) do {} while (0)

struct Params { const char* x; const char* wqkv; const char* wo; const char* wg; const char* wu; const char* wd; char* out; char* ws; };

__device__ __forceinline__ u64 flag_load(const char* ws, int idx) { return __hip_atomic_load((const GLBP u64*)(ws + WS_FLAGS) + idx, __ATOMIC_RELAXED, __HIP_MEMORY_SCOPE_AGENT); }
__device__ __forceinline__ void flag_store(char* ws, int idx, u64 v) { __hip_atomic_store((GLBP u64*)(ws + WS_FLAGS) + idx, v, __ATOMIC_RELAXED, __HIP_MEMORY_SCOPE_AGENT); }
__device__ __forceinline__ void publish(char* ws, int idx, u64 tag) {
    WAITV(0); BARRIER();
    if (threadIdx.x == 0) flag_store(ws, idx, tag);
}
template <bool WAIT>
__device__ __forceinline__ void wait_flags(char* ws, int idx, int n, u64 tag) {
    if constexpr (WAIT) {
        if (threadIdx.x < 64) {
            const int i = idx + ((int)threadIdx.x < n ? (int)threadIdx.x : 0);
            for (unsigned spin = 0; spin < (1u << 22); ++spin) {
                const u64 v = flag_load(ws, i);
                if (__all(v == tag)) break;
                __builtin_amdgcn_s_sleep(2);
            }
        }
    }
    BARRIER();
}

__device__ __forceinline__ void waitv_n(int n) {
    if (n <= 0) WAITV(0); else if (n == 1) WAITV(1); else if (n == 2) WAITV(2); else if (n == 3) WAITV(3); else if (n == 4) WAITV(4); else if (n == 5) WAITV(5);
    else if (n == 6) WAITV(6); else if (n == 7) WAITV(7); else if (n == 8) WAITV(8); else if (n == 9) WAITV(9); else WAITV(10);
}
#define SGB(mask, n) __builtin_amdgcn_sched_group_barrier(mask, n, 0)
template <int NM, int NR, int NV>
__device__ __forceinline__ void interleave() {
    if constexpr (NV < 0) {
#pragma unroll
        for (int i = 0; i < NM; ++i) { SGB(0x8, 1); if (3 * i < NR) SGB(0x100, 3); }
    } else {
#pragma unroll
        for (int i = 0; i < NM; ++i) { SGB(0x8, 1); SGB(0x100, (NR + NM - 1) / NM); if (NV) SGB(0x2, (NV + NM - 1) / NM); }
    }
}
template <int N, int D, int NPW, class Issue> __device__ __forceinline__ void loader_loop(Issue issue, u64* dbg = nullptr);
#define TS(t) asm volatile("s_memtime %0\n\ts_waitcnt lgkmcnt(0)" : "=s"(t) :: "memory")
template <int D, int NPW, int SB, int NM, int NR, int NV, class Frag, class Issue, class Read, class Mma, class After>
__device__ __forceinline__ void gemm_loop_rt(const int N, Issue issue, Read read, Mma mma, After after) {
    Frag f0, f1;
    if constexpr (NPW > 0) waitv_n((D - 1) * NPW);
    BARRIER();
    read(f0, 0, 0, 0);
    int rs = 0;
#pragma nounroll
    for (int s = 0; s < N - D; ++s) {
        const int ns = rs == D - 1 ? 0 : rs + 1;
        read(f1, rs * SB, 1, s); mma(f0, 0); interleave<NM, NR, NV>(); SBAR();
        if constexpr (NPW > 0) waitv_n((D - 2) * NPW);
        BARRIER();
        issue(s + D, rs); SBAR();
        read(f0, ns * SB, 0, s + 1); mma(f1, 1); interleave<NM, NR, NV>(); SBAR();
        after(s);
        rs = ns;
    }
#pragma unroll
    for (int t = 0; t < D; ++t) {
        const int ns = rs == D - 1 ? 0 : rs + 1;
        read(f1, rs * SB, 1, N - D + t); mma(f0, 0); interleave<NM, NR, NV>(); SBAR();
        if (t < D - 1) { if constexpr (NPW > 0) waitv_n((D - 2 - t) * NPW); BARRIER(); read(f0, ns * SB, 0, N - D + t + 1); }
        mma(f1, 1); if (t < D - 1) interleave<NM, NR, NV>(); SBAR();
        rs = ns;
    }
}
template <int D, int NPW, class Issue>
__device__ __forceinline__ void loader_loop_rt(const int N, Issue issue) {
    waitv_n((D - 1) * NPW); BARRIER();
    int rs = 0;
#pragma nounroll
    for (int s = 0; s < N - D; ++s) {
        waitv_n((D - 2) * NPW); BARRIER();
        issue(s + D, rs);
        rs = rs == D - 1 ? 0 : rs + 1;
    }
#pragma unroll
    for (int t = 0; t < D - 1; ++t) { waitv_n((D - 2 - t) * NPW); BARRIER(); }
}
template <int N, int D, int NPW, int SB, int NM, int NR, int NV, class Frag, class Issue, class Read, class Mma>
__device__ __forceinline__ void gemm_loop(Issue issue, Read read, Mma mma, u64* dbg = nullptr) {
    Frag f0, f1;
    waitv_n((D - 1) * NPW); BARRIER();
    read(f0, 0, 0, 0);
    int rs = 0;
#pragma nounroll
    for (int s = 0; s < N - D; ++s) {
        const int ns = rs == D - 1 ? 0 : rs + 1;
        read(f1, rs * SB, 1, s); mma(f0, 0); interleave<NM, NR, NV>(); SBAR();
        waitv_n((D - 2) * NPW); BARRIER();
        issue(s + D, rs); SBAR();
        read(f0, ns * SB, 0, s + 1); mma(f1, 1); interleave<NM, NR, NV>(); SBAR();
        rs = ns;
    }
#pragma unroll
    for (int t = 0; t < D; ++t) {
        const int ns = rs == D - 1 ? 0 : rs + 1;
        read(f1, rs * SB, 1, N - D + t); mma(f0, 0); interleave<NM, NR, NV>(); SBAR();
        if (t < D - 1) { waitv_n((D - 2 - t) * NPW); BARRIER(); read(f0, ns * SB, 0, N - D + t + 1); }
        mma(f1, 1); if (t < D - 1) interleave<NM, NR, NV>(); SBAR();
        rs = ns;
    }
}
template <int N, int D, int SB, int NM, int NR, int NV, class Frag, class Read, class Mma>
__device__ __forceinline__ void cons_loop(Read read, Mma mma, char* wsp = nullptr, int slot0 = 0) {
    static_assert(N % 2 == 0, "");
    Frag fa, fb;
    BARRIER();
    read(fa, 0, 0);
    int ns = 1;
#pragma nounroll
    for (int s = 0; s < N - 2; s += 2) {
        BARRIER();
        read(fb, ns * SB, s + 1); mma(fa); interleave<NM, NR, NV>(); SBAR();
        ns = ns == D - 1 ? 0 : ns + 1;
        BARRIER();
        read(fa, ns * SB, s + 2); mma(fb); interleave<NM, NR, NV>(); SBAR();
        ns = ns == D - 1 ? 0 : ns + 1;
    }
    BARRIER();
    read(fb, ns * SB, N - 1); mma(fa); interleave<NM, NR, NV>(); SBAR();
    mma(fb); SBAR();
}
template <int N, int D, int NPW, class Issue>
__device__ __forceinline__ void loader_loop(Issue issue, u64* dbg) {
    waitv_n((D - 1) * NPW); BARRIER();
    int rs = 0;
#pragma nounroll
    for (int s = 0; s < N - D; ++s) {
        waitv_n((D - 2) * NPW);
        BARRIER();
        issue(s + D, rs);
        rs = rs == D - 1 ? 0 : rs + 1;
    }
#pragma unroll
    for (int t = 0; t < D - 1; ++t) { waitv_n((D - 2 - t) * NPW); BARRIER(); }
}
struct Frag2 { bf16x8 a, b; };
struct Frag3 { bf16x8 a, b, c; };
struct Frag4 { bf16x8 a, b, c, d; };
__device__ __forceinline__ float sum2x4(bf16x8 v, float acc) { const u32x4 u = __builtin_bit_cast(u32x4, v);
    acc = dot2(u[0], 0x3f803f80u, acc); acc = dot2(u[1], 0x3f803f80u, acc); acc = dot2(u[2], 0x3f803f80u, acc); return dot2(u[3], 0x3f803f80u, acc); }
__device__ __forceinline__ float sq2x4(bf16x8 v, float acc) { const u32x4 u = __builtin_bit_cast(u32x4, v);
    acc = dot2(u[0], u[0], acc); acc = dot2(u[1], u[1], acc); acc = dot2(u[2], u[2], acc); return dot2(u[3], u[3], acc); }

constexpr int Q_ST = 4, Q_WB = 12288, Q_A0 = Q_ST * Q_WB, Q_ACH = 8192, Q_SC = Q_A0 + 3 * Q_ACH;
template <bool FUSED>
__device__ __forceinline__ void phase_q(const Params& P, char* lds, u64 tag) {
    const int tid = threadIdx.x, lane = tid & 63, r = lane & 31, hh = lane >> 5;
    const int w = __builtin_amdgcn_readfirstlane(tid >> 6);
    const int b = blockIdx.x & 255, xcd = b & 7, bi = b >> 3;
    const int ri = bi & 7, cj = bi >> 3;
    const int rb = 8 * (2 * (xcd & 1) + ((xcd >> 1) & 1)) + ri, cg = 4 * (xcd >> 2) + cj;
    const int kc0 = 0;
    const int m0 = rb * 64;
    const unsigned lds0 = (unsigned)(uintptr_t)lds;
    float* sc = (float*)(lds + Q_SC);
    if (w >= 4) {
        const int p = w - 4;
        unsigned woff[3];
        unsigned aoff0;
        { const int row = 16 * p + (lane >> 3), c = (lane & 7) ^ ((row >> 1) & 7); aoff0 = row * 1024 + c * 16; }
#pragma unroll
        for (int i = 0; i < 3; ++i) { const int pc = 64 * (3 * p + i) + lane, row = pc / 24, ch = pc % 24, cs = (ch & 7) ^ (((row >> 1) & 1) << 2); woff[i] = row * 3072 + (ch >> 3) * 1024 + cg * 128 + cs * 16; }
        woff[1] -= 1024; woff[2] -= 2048;
        const char* ag = uni(P.x + (size_t)m0 * 1024);
        const char* wq = uni(P.wqkv);
        const unsigned ldsW = uni(lds0 + 3 * p * 1024), ldsX = uni(lds0 + Q_A0 + 2 * p * 1024);
        auto issueW = [&](int st, int slot) { const int kt = (2 * kc0 + st) & 15; gldsN<false, 3>(wq + kt * (32 * 3072), ldsW + slot * Q_WB, woff[0], woff[1], woff[2]); };
        auto issueX = [&](int c, int cslot, int set) { const int kc = (kc0 + c) & 7; glds16<false>(ag + kc * 128 + set * 8192, aoff0 ^ (unsigned)(set << 6), ldsX + cslot * Q_ACH + set * 1024); };
        issueX(0, 0, 0); issueX(0, 0, 1); issueW(0, 0); issueX(1, 1, 0); issueX(1, 1, 1); issueW(1, 1); issueW(2, 2); issueX(2, 2, 0); issueX(2, 2, 1); issueW(3, 3);
        loader_loop<16, Q_ST, 4>([&](int st, int slot) {
            const int u = st + 1; const int c = (u >> 1) < 7 ? (u >> 1) : 7;
            issueW(st, slot); issueX(c, c % 3, u & 1); }, (u64*)(P.ws + (40u << 20)) + (size_t)(blockIdx.x * 8 + w) * 64);
        {
            const unsigned tg = (unsigned)tag;
            if (p == 0) {
                u32x4 v;
                for (unsigned spin = 0; spin < (1u << 16); ++spin) {
                    asm volatile("s_nop 4\n\tglobal_load_dwordx4 %0, %1, %2 sc1\n\ts_waitcnt vmcnt(0)" : "=v"(v) : "v"((unsigned)(m0 + lane) * 16), "s"(uni(P.ws + WS_GST)) : "memory");
                    if (__all(v[1] == tg && v[3] == tg)) break;
                    __builtin_amdgcn_s_sleep(1);
                }
                *(f32x2*)&sc[lane * 2] = (f32x2){__uint_as_float(v[0]), __uint_as_float(v[2])};
            } else {
                u32x4 v0, v1;
                const unsigned off = (unsigned)(((lane >> 5) * 2) * 1536 + (p - 1) * 512 + cg * 64 + 2 * (lane & 31)) * 8;
                for (unsigned spin = 0; spin < (1u << 16); ++spin) {
                    asm volatile("s_nop 4\n\tglobal_load_dwordx4 %0, %2, %3 sc1\n\tglobal_load_dwordx4 %1, %4, %3 sc1\n\ts_waitcnt vmcnt(0)" : "=&v"(v0), "=&v"(v1) : "v"(off), "s"(uni(P.ws + WS_GCS)), "v"(off + 1536 * 8) : "memory");
                    if (__all(v0[1] == tg && v0[3] == tg && v1[1] == tg && v1[3] == tg)) break;
                    __builtin_amdgcn_s_sleep(1);
                }
                const float c0 = swap_add(__uint_as_float(v0[0]) + __uint_as_float(v1[0])), c1 = swap_add(__uint_as_float(v0[2]) + __uint_as_float(v1[2]));
                if (lane < 32) *(f32x2*)&sc[128 + (p - 1) * 64 + 2 * lane] = (f32x2){c0, c1};
            }
        }
        BARRIER();
        STAMP(2);
    } else {
        const int rt = w >> 1, hf = w & 1;
        const int arow = 32 * rt + r;
        unsigned a_rd[2];
#pragma unroll
        for (int k = 0; k < 2; ++k) a_rd[k] = Q_A0 + arow * 128 + ((2 * k + hh) ^ ((arow >> 1) & 7)) * 16;
        const int q4 = (lane & 15) >> 2;
        const unsigned chw = (4 * hf + 2 * ((lane >> 4) & 1) + ((lane & 3) >> 1)) ^ (((q4 >> 1) & 1) << 2);
        const unsigned w_rd = (8 * hh + q4) * 384 + chw * 16 + 8 * (lane & 1);
        unsigned pf0;
        {
            const int idx = w * 64 + lane;
            const char* a;
            if (idx < 144) { const int st = 4 + idx / 12, l = ri * 12 + idx % 12; a = P.wqkv + (size_t)(st * 32 + l / 3) * 3072 + (l % 3) * 1024 + cg * 128; }
            else { const int i2 = idx < 224 ? idx - 144 : 0; a = P.x + (size_t)(m0 + cj * 16 + (i2 & 15)) * 1024 + (3 + (i2 >> 4)) * 128; }
            pf0 = 0;
        }
        f32x16 accQ = {}, accK = {}, accV = {};
        struct FragQ { Frag4 k0, k1; };
        auto rd = [&](Frag4& f, lds_cptr xa, lds_cptr wb) { f.a = ldsr128(xa);
            f.b = cat8(vtr(wb), vtr(wb + 1536)); f.c = cat8(vtr(wb + 128), vtr(wb + 128 + 1536)); f.d = cat8(vtr(wb + 256), vtr(wb + 256 + 1536)); };
        cons_loop<16, Q_ST, Q_WB, 6, 14, 0, FragQ>(
            [&](FragQ& f, int so, int stg) { lds_cptr sb = (lds_cptr)lds + so + w_rd; lds_cptr xb = (lds_cptr)lds + ((stg >> 1) % 3) * Q_ACH; const unsigned fl = (stg & 1) << 6;
                rd(f.k0, xb + (a_rd[0] ^ fl), sb); rd(f.k1, xb + (a_rd[1] ^ fl), sb + 6144); },
            [&](const FragQ& f) {
                accQ = MFMA32(f.k0.b, f.k0.a, accQ); accK = MFMA32(f.k0.c, f.k0.a, accK); accV = MFMA32(f.k0.a, f.k0.d, accV);
                accQ = MFMA32(f.k1.b, f.k1.a, accQ); accK = MFMA32(f.k1.c, f.k1.a, accK); accV = MFMA32(f.k1.a, f.k1.d, accV);
                PIN(accQ); PIN(accK); PIN(accV); }, P.ws, 1);
        STAMP(13);
        asm volatile("s_waitcnt vmcnt(0)" : "+v"(pf0) :: "memory");
        BARRIER(); SBAR();
        STAMP(2);
        const int tile = rb * 2 + rt, hd0 = cg * 4 + hf * 2;
        {
            const f32x2 st = *(const f32x2*)&sc[arow * 2];
            const float mu = st[0], rstd = st[1];
#pragma unroll
            for (int isk = 0; isk < 2; ++isk) {
                const f32x16& acc = isk ? accK : accQ;
                const float scl = isk ? rstd : rstd * QSCALE;
                unsigned pk[8];
#pragma unroll
                for (int q = 0; q < 4; ++q) {
                    const f32x4 c4 = *(const f32x4*)&sc[128 + isk * 64 + hf * 32 + 8 * q + 4 * hh];
                    const float v0 = (acc[4 * q] - mu * c4[0]) * scl, v1 = (acc[4 * q + 1] - mu * c4[1]) * scl, v2 = (acc[4 * q + 2] - mu * c4[2]) * scl, v3 = (acc[4 * q + 3] - mu * c4[3]) * scl;
                    pk[2 * q] = cvtpk(v0, v1); pk[2 * q + 1] = cvtpk(v2, v3); }
                char* dst = P.ws + (isk ? WS_K : WS_Q);
#pragma unroll
                for (int hd = 0; hd < 2; ++hd) st16_sc1(dst, (unsigned)(((hd0 + hd) * 64 + tile) * 64 + lane) * 16, (u32x4){pk[4 * hd], pk[4 * hd + 1], pk[4 * hd + 2], pk[4 * hd + 3]});
                SBAR();
            }
        }
        {
            const float csvv = sc[128 + 128 + hf * 32 + r];
            unsigned pk[8];
#pragma unroll
            for (int q = 0; q < 4; ++q) { const f32x4 s0 = *(const f32x4*)&sc[(32 * rt + 8 * q + 4 * hh) * 2], s1 = *(const f32x4*)&sc[(32 * rt + 8 * q + 4 * hh) * 2 + 4];
                const float v0 = (accV[4 * q] - s0[0] * csvv) * s0[1], v1 = (accV[4 * q + 1] - s0[2] * csvv) * s0[3], v2 = (accV[4 * q + 2] - s1[0] * csvv) * s1[1], v3 = (accV[4 * q + 3] - s1[2] * csvv) * s1[3];
                pk[2 * q] = cvtpk(v0, v1); pk[2 * q + 1] = cvtpk(v2, v3); }
            const int head = hd0 + (r >> 4), d = r & 15;
#pragma unroll
            for (int s = 0; s < 2; ++s) st16_sc1(P.ws + WS_V, (unsigned)((((head * 128 + tile * 2 + s) * 2 + hh) * 16 + d) * 16), (u32x4){pk[4 * s], pk[4 * s + 1], pk[4 * s + 2], pk[4 * s + 3]});
        }
    }
    if constexpr (FUSED) { publish(P.ws, FL_Q + cg * 32 + rb, tag); if (threadIdx.x == 0) flag_store(P.ws, FL_START + blockIdx.x, tag); } else { WAITV(0); BARRIER(); }
    STAMP(3);
}

constexpr int G_ST = 5, G_AB = 8192, G_SB = 16384;
template <bool FUSED>
__device__ __forceinline__ void phase_u(const Params& P, char* lds, u64 tag) {
    const int tid = threadIdx.x, lane = tid & 63, r = lane & 31, hh = lane >> 5;
    const int w = __builtin_amdgcn_readfirstlane(tid >> 6), wm = (w >> 1) & 1, wn = w & 1;
    const int b = blockIdx.x & 255, xcd = b & 7, bi = b >> 3;
    const int ntile = bi < 24 ? 2 : 1;
    const int rb = 8 * (xcd & 1) + (bi & 7), cb0 = 7 * (xcd >> 1) + (bi >> 3);
    const int m0 = rb * 128;
    const unsigned lds0 = (unsigned)(uintptr_t)lds;
    wait_flags<FUSED>(P.ws, FL_S + 16 * rb, 16, tag);
    STAMP(4);
    if (w >= 4) {
        const int p = w & 3;
        unsigned woff;
        { const int row = 8 * p + (lane >> 3), ch = (lane & 7) ^ (((row >> 1) & 1) << 2); woff = row * (DF * 2) + cb0 * 128 + ch * 16; }
        const char* ag = uni(P.ws + WS_XN + (size_t)m0 * 64 + p * 2048);
        const char* wgp = uni(P.wg); const char* wup = uni(P.wu);
        const unsigned ldsA = uni(lds0 + 2 * p * 1024), ldsW = uni(lds0 + G_AB + p * 1024);
        auto issue = [&](int st, int slot) { const int kt = st & 15; const int wo = kt * (32 * DF * 2) + (st >> 4) * 512;
            gldsN<true, 2>(ag + kt * (2048 * 64), ldsA + slot * G_SB, lane * 16, lane * 16);
            glds16<false>(wgp + wo, woff, ldsW + slot * G_SB); glds16<false>(wup + wo, woff, ldsW + 4096 + slot * G_SB); };
#pragma unroll
        for (int i = 0; i < G_ST; ++i) issue(i, i);
        loader_loop_rt<G_ST, 4>(16 * ntile, issue);
    } else {
        f32x16 acc[2][2] = {};
        unsigned a_rd[2];
        { const int arow = 64 * wm + r; a_rd[0] = arow * 64 + ((0 + hh) ^ ((arow >> 2) & 3)) * 16; a_rd[1] = arow * 64 + ((2 + hh) ^ ((arow >> 2) & 3)) * 16; }
        const int q4 = (lane & 15) >> 2;
        const unsigned chg = (4 * wn + 2 * ((lane >> 4) & 1) + ((lane & 3) >> 1)) ^ (((q4 >> 1) & 1) << 2);
        const unsigned w_rd = G_AB + (8 * hh + q4) * 128 + chg * 16 + 8 * (lane & 1);
        auto epilogue = [&](int cb) {
            constexpr float GA = -2.f * 0.7978845608028654f * 1.4426950408889634f, GB = GA * 0.044715f;
#pragma unroll
            for (int i = 0; i < 2; ++i) {
                unsigned pk[8];
#pragma unroll
                for (int q = 0; q < 4; ++q) {
                    float hv[4];
#pragma unroll
                    for (int j = 0; j < 4; ++j) {
                        const float gv = acc[i][0][4 * q + j], uv = acc[i][1][4 * q + j];
                        const float z = gv * (GA + GB * gv * gv);
                        hv[j] = gv * uv * __builtin_amdgcn_rcpf(1.f + __builtin_amdgcn_exp2f(z));
                    }
                    pk[2 * q] = cvtpk(hv[0], hv[1]); pk[2 * q + 1] = cvtpk(hv[2], hv[3]);
                }
                const unsigned off = (unsigned)(m0 + 64 * wm + 32 * i + r) * (DF * 2) + (cb * 64 + 32 * wn) * 2 + hh * 16;
                st16_sc1(P.ws + WS_H, off, (u32x4){pk[0], pk[1], pk[2], pk[3]}); st16_sc1(P.ws + WS_H, off + 32, (u32x4){pk[4], pk[5], pk[6], pk[7]});
            } };
        gemm_loop_rt<G_ST, 0, G_SB, 4, 6, -1, Frag4>(16 * ntile, [&](int, int) {},
            [&](Frag4& f, int so, int kk, int) { lds_cptr sb = (lds_cptr)lds + so;
                f.c = ldsr128(sb + a_rd[kk]);
                f.a = cat8(vtr(sb + w_rd + kk * 2048), vtr(sb + w_rd + kk * 2048 + 512));
                f.b = cat8(vtr(sb + w_rd + 4096 + kk * 2048), vtr(sb + w_rd + 4096 + kk * 2048 + 512));
                f.d = ldsr128(sb + a_rd[kk] + 2048); },
            [&](const Frag4& f, int) {
                acc[0][0] = MFMA32(f.a, f.c, acc[0][0]); acc[0][1] = MFMA32(f.b, f.c, acc[0][1]);
                acc[1][0] = MFMA32(f.a, f.d, acc[1][0]); acc[1][1] = MFMA32(f.b, f.d, acc[1][1]); PIN(acc[0][0]); PIN(acc[0][1]); PIN(acc[1][0]); PIN(acc[1][1]); },
            [&](int s) { if (s == 15) { epilogue(cb0); acc[0][0] = (f32x16){}; acc[0][1] = (f32x16){}; acc[1][0] = (f32x16){}; acc[1][1] = (f32x16){}; SBAR(); } });
        STAMP(5);
        epilogue(cb0 + 4 * (ntile - 1));
    }
    if constexpr (FUSED) {
        WAITV(0); BARRIER();
        if (threadIdx.x == 0) { flag_store(P.ws, FL_H + rb * 32 + cb0, tag); if (ntile == 2) flag_store(P.ws, FL_H + rb * 32 + cb0 + 4, tag); }
    } else { WAITV(0); BARRIER(); }
    STAMP(6);
}

constexpr int D_ST = 4, D_AB = 8192, D_SB = 16384;
template <bool FUSED>
__device__ __forceinline__ void phase_d(const Params& P, char* lds, u64 tag) {
    const int tid = threadIdx.x, lane = tid & 63, r = lane & 31, hh = lane >> 5;
    const int w = __builtin_amdgcn_readfirstlane(tid >> 6), wk = w >> 2, wm = (w >> 1) & 1, wn = w & 1;
    const int b = blockIdx.x & 255, xcd = b & 7, bi = b >> 3, rb = xcd * 4 + (bi & 3), cb = bi >> 2;
    const int m0 = rb * 64, n0 = cb * 64;
    const int kt0 = 0;
    const unsigned lds0 = (unsigned)(uintptr_t)lds;
    constexpr int NT = 28;
    const int row = m0 + 32 * wm + r;
    u32x2 xv[2];
#pragma unroll
    for (int q = 0; q < 2; ++q) xv[q] = *(const u32x2*)(P.x + (size_t)row * 1024 + (n0 + 32 * wn + 4 * hh + 8 * (2 * wk + q)) * 2);
    const int q4 = (lane & 15) >> 2;
    const unsigned chw = (4 * wn + 2 * ((lane >> 4) & 1) + ((lane & 3) >> 1)) ^ (((q4 >> 1) & 1) << 2);
    const unsigned w_rd = (4 * hh + q4) * 128 + chw * 16 + 8 * (lane & 1);
    f32x16 acc = {};
    if (wk == 1) {
        const int p = w & 3;
        unsigned aoff[2], woff2[2];
#pragma unroll
        for (int i = 0; i < 2; ++i) { const int rw = 8 * (2 * p + i) + (lane >> 3), c = (lane & 7) ^ ((rw >> 1) & 7); aoff[i] = rw * (DF * 2) + c * 16 - 1024 * i;
            const int ch = (lane & 7) ^ (((rw >> 1) & 1) << 2); woff2[i] = rw * 1024 + n0 * 2 + ch * 16 - 1024 * i; }
        const char* hg = uni(P.ws + WS_H + (size_t)m0 * (DF * 2)); const char* wd = uni(P.wd);
        const unsigned ldsA = uni(lds0 + 2 * p * 1024), ldsW = uni(lds0 + D_AB + 2 * p * 1024);
        auto issue_w = [&](int it, int slot) { int kt = kt0 + it; if (kt >= NT) kt -= NT; gldsN<false, 2>(wd + kt * (64 * 1024), ldsW + slot * D_SB, woff2[0], woff2[1]); };
        auto issue_a = [&](int it, int slot) { int kt = kt0 + it; if (kt >= NT) kt -= NT; gldsN<true, 2>(hg + kt * 128, ldsA + slot * D_SB, aoff[0], aoff[1]); };
#pragma unroll
        for (int i = 0; i < D_ST; ++i) issue_w(i, i);
        wait_flags<FUSED>(P.ws, FL_H + (rb >> 1) * 32, 28, tag);
#pragma unroll
        for (int i = 0; i < D_ST; ++i) { issue_a(i, i); issue_w(i, i); }
        loader_loop<NT, D_ST, 4>([&](int it, int slot) { issue_a(it, slot); issue_w(it, slot); });
    } else {
        wait_flags<FUSED>(P.ws, FL_H + (rb >> 1) * 32, 28, tag);
        STAMP(7);
        const int arow = 32 * wm + r;
        unsigned a_rd[4];
#pragma unroll
        for (int kk = 0; kk < 4; ++kk) a_rd[kk] = arow * 128 + ((2 * kk + hh) ^ ((arow >> 1) & 7)) * 16;
        struct FragD { Frag4 k0, k1; };
        auto rd = [&](Frag4& f, lds_cptr sb, int k) {
            f.a = cat8(vtr(sb + D_AB + w_rd + (2 * k) * 2048), vtr(sb + D_AB + w_rd + (2 * k) * 2048 + 1024));
            f.b = ldsr128(sb + a_rd[2 * k]);
            f.c = cat8(vtr(sb + D_AB + w_rd + (2 * k + 1) * 2048), vtr(sb + D_AB + w_rd + (2 * k + 1) * 2048 + 1024));
            f.d = ldsr128(sb + a_rd[2 * k + 1]); };
        cons_loop<NT, D_ST, D_SB, 4, 12, 0, FragD>(
            [&](FragD& f, int so, int) { lds_cptr sb = (lds_cptr)lds + so; rd(f.k0, sb, 0); rd(f.k1, sb, 1); },
            [&](const FragD& f) { acc = MFMA32(f.k0.a, f.k0.b, acc); acc = MFMA32(f.k0.c, f.k0.d, acc); acc = MFMA32(f.k1.a, f.k1.b, acc); acc = MFMA32(f.k1.c, f.k1.d, acc); PIN(acc); });
    }
    unsigned woff;
    { const int rw = 8 * w + (lane >> 3); const int ch = (lane & 7) ^ (((rw >> 1) & 1) << 2); woff = rw * 1024 + n0 * 2 + ch * 16; }
    BARRIER();
    STAMP(8);
#pragma unroll
    for (int i = 0; i < 8; ++i) glds16<false>(P.wo + (size_t)i * 64 * 1024, woff, lds0 + i * 8192 + w * 1024);
    float* ex = (float*)(lds + 65536);
    if (wk == 0) {
#pragma unroll
        for (int j = 0; j < 8; ++j) ex[(w * 8 + j) * 64 + lane] = acc[8 + j];
    }
    BARRIER();
    float gate[8];
#pragma unroll
    for (int j = 0; j < 8; ++j) { const float yv = wk ? ex[((w ^ 4) * 8 + j) * 64 + lane] : acc[j]; gate[j] = yv * __builtin_amdgcn_rcpf(1.f + __builtin_amdgcn_exp2f(-1.4426950408889634f * yv)); }
    if constexpr (FUSED) {
        if (blockIdx.x == 0 && w == 7) {
            bool ok = false;
            for (unsigned spin = 0; spin < (1u << 16) && !ok; ++spin) { bool a = true;
#pragma unroll
                for (int i = 0; i < 8; ++i) a &= flag_load(P.ws, FL_START + i * 64 + lane) == tag;
                ok = __all(a); if (!ok) __builtin_amdgcn_s_sleep(8); }
            if (lane == 0) flag_store(P.ws, FL_GEN, tag);
        }
    }
    WAITV(0);
    wait_flags<FUSED>(P.ws, FL_O + (rb >> 2) * 32, 32, tag);
    STAMP(9);
    u32x4 of[16];
    {
        const char* og = P.ws + WS_O + ((size_t)(rb * 2 + wm) * 32 + 16 * wk) * 1024;
#pragma unroll
        for (int i = 0; i < 16; ++i) asm volatile("s_nop 4\n\tglobal_load_dwordx4 %0, %1, %2 offset:%3 sc1" : "=v"(of[i]) : "v"(lane * 16 + (i >> 2) * 4096), "s"(uni(og)), "n"((i & 3) * 1024) : "memory");
    }
    f32x16 accS = {};
#pragma unroll
    for (int i = 0; i < 16; ++i) {
        const bf16x8 wb = cat8(vtr((lds_cptr)lds + w_rd + (16 * wk + i) * 2048), vtr((lds_cptr)lds + w_rd + (16 * wk + i) * 2048 + 1024));
        if (i == 0) asm volatile("s_waitcnt vmcnt(12)" : "+v"(of[0]), "+v"(of[1]), "+v"(of[2]), "+v"(of[3]) :: "memory");
        if (i == 4) asm volatile("s_waitcnt vmcnt(8)" : "+v"(of[4]), "+v"(of[5]), "+v"(of[6]), "+v"(of[7]) :: "memory");
        if (i == 8) asm volatile("s_waitcnt vmcnt(4)" : "+v"(of[8]), "+v"(of[9]), "+v"(of[10]), "+v"(of[11]) :: "memory");
        if (i == 12) asm volatile("s_waitcnt vmcnt(0)" : "+v"(of[12]), "+v"(of[13]), "+v"(of[14]), "+v"(of[15]) :: "memory");
        accS = MFMA32(wb, __builtin_bit_cast(bf16x8, of[i]), accS);
        if ((i & 1) == 1) SBAR();
    }
#pragma unroll
    for (int j = 0; j < 8; ++j) ex[(w * 8 + j) * 64 + lane] = accS[8 * (1 - wk) + j];
    BARRIER();
    char* orow = P.out + (size_t)row * 1024 + (n0 + 32 * wn + 4 * hh + 16 * wk) * 2;
#pragma unroll
    for (int q = 0; q < 2; ++q) {
        float res[4];
#pragma unroll
        for (int j = 0; j < 4; ++j) {
            const float sv = accS[8 * wk + 4 * q + j] + ex[((w ^ 4) * 8 + 4 * q + j) * 64 + lane];
            const float xx = (j & 1) ? bfhi(xv[q][j >> 1]) : bflo(xv[q][j >> 1]);
            res[j] = xx + sv * gate[4 * q + j];
        }
        *(u32x2*)(orow + q * 16) = (u32x2){cvtpk(res[0], res[1]), cvtpk(res[2], res[3])};
    }
    STAMP(10);
}

constexpr float REF_BIAS = 88.f;
__device__ __forceinline__ void attn_slow(const char* kf, const char* vf, const bf16x8 qf, f32x16& o, int lane) {
    const int r = lane & 31;
    float m = -INFINITY; f32x16 oa = {};
    for (int t = 0; t < 64; ++t) {
        u32x4 kq, va0, va1;
        asm volatile("s_nop 4\n\tglobal_load_dwordx4 %0, %3, %4 sc1\n\tglobal_load_dwordx4 %1, %5, %6 sc1\n\tglobal_load_dwordx4 %2, %5, %6 offset:512 sc1\n\ts_waitcnt vmcnt(0)"
                     : "=&v"(kq), "=&v"(va0), "=&v"(va1) : "v"(t * 1024 + lane * 16), "s"(uni(kf)), "v"(t * 1024 + (lane >> 5) * 256 + (r & 15) * 16), "s"(uni(vf)) : "memory");
        f32x16 s = MFMA32(__builtin_bit_cast(bf16x8, kq), qf, (f32x16){});
        float tm = s[0];
#pragma unroll
        for (int i = 1; i < 16; ++i) tm = fmaxf(tm, s[i]);
        tm = swap_max(tm);
        const float mn = fmaxf(m, tm);
        const float al = __builtin_amdgcn_exp2f(m - mn);
        m = mn;
#pragma unroll
        for (int i = 0; i < 16; ++i) oa[i] *= al;
        unsigned pk[8];
#pragma unroll
        for (int i = 0; i < 8; ++i) pk[i] = cvtpk(__builtin_amdgcn_exp2f(s[2 * i] - mn), __builtin_amdgcn_exp2f(s[2 * i + 1] - mn));
        if (r >= 16) { const unsigned c = (r == 16 || r == 20) ? 0x3f803f80u : 0u; va0 = (u32x4){c, c, c, c}; va1 = va0; }
        oa = MFMA32(__builtin_bit_cast(bf16x8, va0), __builtin_bit_cast(bf16x8, ((u32x4){pk[0], pk[1], pk[2], pk[3]})), oa);
        oa = MFMA32(__builtin_bit_cast(bf16x8, va1), __builtin_bit_cast(bf16x8, ((u32x4){pk[4], pk[5], pk[6], pk[7]})), oa);
    }
    o = oa;
}

constexpr int A_SLOT = 16384, A_CONST = 65536;
template <bool FUSED>
__device__ __forceinline__ void phase_attn(const Params& P, char* lds, u64 tag) {
    const int tid = threadIdx.x, lane = tid & 63, r = lane & 31, hh = lane >> 5;
    const int w = __builtin_amdgcn_readfirstlane(tid >> 6);
    const int b = blockIdx.x & 255, xcd = b & 7, bi = b >> 3, head = xcd * 4 + (bi & 3), qb = bi >> 2;
    const unsigned lds0 = (unsigned)(uintptr_t)lds;
    const char* kf = P.ws + WS_K + (size_t)head * 65536; const char* vf = P.ws + WS_V + (size_t)head * 65536;
    { const int row = tid & 15; const unsigned c = (row == 0 || row == 4) ? 0x3f803f80u : 0u; *(u32x4*)(lds + A_CONST + tid * 16) = (u32x4){c, c, c, c}; }
    if constexpr (FUSED) { PIN(tag); WAITV(0); }
    wait_flags<FUSED>(P.ws, FL_Q + (head >> 2) * 32, 32, tag);
    if constexpr (FUSED) { if (threadIdx.x == 0) flag_store(P.ws, FL_START + blockIdx.x, tag); }
    STAMP(1);
    auto issue = [&](int ci) {
        const int c = (qb + ci) & 7; const unsigned sb = lds0 + (ci & 3) * A_SLOT;
        glds16<true>(kf + c * 8192 + w * 1024, lane * 16, sb + w * 1024);
        glds16<true>(vf + c * 8192 + w * 1024, lane * 16, sb + 8192 + w * 1024);
    };
    issue(0);
    u32x4 qv;
    asm volatile("s_nop 4\n\tglobal_load_dwordx4 %0, %1, %2 sc1" : "=v"(qv) : "v"(lane * 16), "s"(uni(P.ws + WS_Q + (size_t)(head * 64 + qb * 8 + w) * 1024)) : "memory");
    issue(1); issue(2); issue(3);
    asm volatile("s_waitcnt vmcnt(6)" : "+v"(qv) :: "memory");
    BARRIER();
    STAMP(2);
    const bf16x8 qf = __builtin_bit_cast(bf16x8, qv);
    const unsigned kaddr = lane * 16;
    const unsigned vaddr = (r < 16) ? (8192 + hh * 256 + r * 16) : (A_CONST + hh * 256 + (r - 16) * 16);
    const unsigned vmul = (r < 16) ? 1u : 0u;
    f32x16 o, cneg, sA, sB;
    {
        const bf16x8 k0 = ldsr128((lds_cptr)lds + kaddr), k1 = ldsr128((lds_cptr)lds + kaddr + 1024);
        const f32x16 s0 = MFMA32(k0, qf, (f32x16){}), s1 = MFMA32(k1, qf, (f32x16){});
        float tm = fmaxf(s0[0], s1[0]);
#pragma unroll
        for (int i = 1; i < 16; ++i) tm = fmaxf(tm, fmaxf(s0[i], s1[i]));
        tm = swap_max(tm);
        const float c = -(tm + REF_BIAS);
#pragma unroll
        for (int i = 0; i < 16; ++i) { cneg[i] = c; o[i] = 0.f; }
        PIN(cneg);
        sA = MFMA32(k0, qf, cneg);
    }
    lds_cptr kp = (lds_cptr)lds + kaddr;
    lds_cptr vp = (lds_cptr)lds + vaddr;
    bf16x8 kfr = ldsr128(kp + 1024);
    bf16x8 v0f = ldsr128(vp), v1f;
    { u32x4 z = {0u, 0u, 0u, 0u}; v1f = __builtin_bit_cast(bf16x8, z); }
    u32x4 pk0 = {0u, 0u, 0u, 0u}, pk1 = {0u, 0u, 0u, 0u};
#define EXP2(x) __builtin_amdgcn_exp2f(x)
#define EX(dst, src) do { dst = EXP2(src); PIN(dst); } while (0)
#define CV(dst, a, b) do { dst = cvtpk(a, b); PIN(dst); } while (0)
#define BF(x) __builtin_bit_cast(bf16x8, x)
#define STEP(cur, nxt, KN, V1, V0N) do { \
        float e[16]; \
        nxt = MFMA32(kfr, qf, cneg); PIN(nxt); SBAR(); \
        PIN(cur); kfr = ldsr128(KN); SBAR(); \
        EX(e[0], cur[0]); EX(e[1], cur[1]); EX(e[2], cur[2]); EX(e[3], cur[3]); EX(e[4], cur[4]); EX(e[5], cur[5]); EX(e[6], cur[6]); SBAR(); \
        o = MFMA32(v1f, BF(pk1), o); PIN(o); SBAR(); \
        PIN(cur); PIN(e[0]); PIN(e[2]); PIN(e[4]); PIN(e[6]); v1f = ldsr128(V1); SBAR(); \
        EX(e[7], cur[7]); CV(pk0[0], e[0], e[1]); CV(pk0[1], e[2], e[3]); CV(pk0[2], e[4], e[5]); \
        EX(e[8], cur[8]); CV(pk0[3], e[6], e[7]); EX(e[9], cur[9]); EX(e[10], cur[10]); EX(e[11], cur[11]); SBAR(); \
        o = MFMA32(v0f, BF(pk0), o); PIN(o); SBAR(); \
        PIN(cur); PIN(e[8]); PIN(e[10]); v0f = ldsr128(V0N); SBAR(); \
        EX(e[12], cur[12]); EX(e[13], cur[13]); EX(e[14], cur[14]); EX(e[15], cur[15]); \
        CV(pk1[0], e[8], e[9]); CV(pk1[1], e[10], e[11]); CV(pk1[2], e[12], e[13]); CV(pk1[3], e[14], e[15]); SBAR(); \
    } while (0)
    for (int ci = 0; ci < 8; ++ci) {
        lds_cptr kc = kp + (ci & 3) * A_SLOT, kn = kp + ((ci + 1) & 3) * A_SLOT;
        lds_cptr vc = vp + vmul * ((ci & 3) * A_SLOT), vn = vp + vmul * (((ci + 1) & 3) * A_SLOT);
        STEP(sA, sB, kc + 2 * 1024, vc + 0 * 1024 + 512, vc + 1 * 1024);
        STEP(sB, sA, kc + 3 * 1024, vc + 1 * 1024 + 512, vc + 2 * 1024);
        STEP(sA, sB, kc + 4 * 1024, vc + 2 * 1024 + 512, vc + 3 * 1024);
        STEP(sB, sA, kc + 5 * 1024, vc + 3 * 1024 + 512, vc + 4 * 1024);
        STEP(sA, sB, kc + 6 * 1024, vc + 4 * 1024 + 512, vc + 5 * 1024);
        STEP(sB, sA, kc + 7 * 1024, vc + 5 * 1024 + 512, vc + 6 * 1024);
        if (ci < 5) WAITV(2); else WAITV(0);
        BARRIER();
        if (ci >= 1 && ci + 3 < 8) issue(ci + 3);
        STEP(sA, sB, kn + 0 * 1024, vc + 6 * 1024 + 512, vc + 7 * 1024);
        STEP(sB, sA, kn + 1 * 1024, vc + 7 * 1024 + 512, vn);
    }
    o = MFMA32(v1f, BF(pk1), o);
    STAMP(3);
    bool bad = !(o[8] < 1.0e37f) || !(o[8] > 0.f);
#pragma unroll
    for (int i = 0; i < 8; ++i) bad |= !(fabsf(o[i]) < 3.0e38f);
    if (__any(bad)) attn_slow(kf, vf, qf, o, lane);
    const float rl = 1.0f / o[8];
    const u32x4 pk = {cvtpk(o[0] * rl, o[1] * rl), cvtpk(o[2] * rl, o[3] * rl), cvtpk(o[4] * rl, o[5] * rl), cvtpk(o[6] * rl, o[7] * rl)};
    st16_sc1(P.ws + WS_O, (unsigned)(((qb * 8 + w) * 32 + head) * 64 + lane) * 16, pk);
    if constexpr (FUSED) publish(P.ws, FL_O + qb * 32 + head, tag);
    STAMP(4);
}

template <bool FUSED>
__device__ __forceinline__ void phase_side(const Params& P, char* lds, u64 tag) {
    const int tid = threadIdx.x, lane = tid & 63;
    const int w = __builtin_amdgcn_readfirstlane(tid >> 6);
    const int j = blockIdx.x & 255;
    const int row = 8 * j + w;
    const unsigned tg = (unsigned)tag;
    const u32x4 xr = *(const u32x4*)(P.x + (size_t)row * 1024 + lane * 16);
    const bool has = j < 96;
    const int cgp = j % 24, sl = j / 24;
    u32x4 wv[2] = {};
    if (has) {
#pragma unroll
        for (int i = 0; i < 2; ++i) wv[i] = *(const u32x4*)(P.wqkv + (size_t)(128 * sl + 16 * w + 8 * i + (lane >> 3)) * 3072 + cgp * 128 + (lane & 7) * 16);
    }
    float v[8];
#pragma unroll
    for (int i = 0; i < 4; ++i) { v[2 * i] = bflo(xr[i]); v[2 * i + 1] = bfhi(xr[i]); }
    float sx = ((v[0] + v[1]) + (v[2] + v[3])) + ((v[4] + v[5]) + (v[6] + v[7]));
    sx = wave_sum(sx);
    STAMP(5);
    const float mu = sx * (1.f / 512.f);
    float sq = 0.f;
#pragma unroll
    for (int i = 0; i < 8; ++i) { v[i] -= mu; sq = fmaf(v[i], v[i], sq); }
    sq = wave_sum(sq);
    const float rstd = __builtin_amdgcn_rsqf(sq * (1.f / 512.f) + 1e-5f);
    if (lane == 0) st16_sc1(P.ws + WS_GST, (unsigned)row * 16, (u32x4){__float_as_uint(mu), tg, __float_as_uint(rstd), tg});
    const u32x4 pk = {cvtpk(v[0] * rstd, v[1] * rstd), cvtpk(v[2] * rstd, v[3] * rstd), cvtpk(v[4] * rstd, v[5] * rstd), cvtpk(v[6] * rstd, v[7] * rstd)};
    st16_sc1(P.ws + WS_XN, (unsigned)((((lane >> 2) * 2048 + row) * 4 + ((lane & 3) ^ ((row >> 2) & 3))) * 16), pk);
    if (has) {
        float a[8];
#pragma unroll
        for (int k = 0; k < 4; ++k) { a[2 * k] = bflo(wv[0][k]) + bflo(wv[1][k]); a[2 * k + 1] = bfhi(wv[0][k]) + bfhi(wv[1][k]); }
#pragma unroll
        for (int k = 0; k < 8; ++k) a[k] = swap_add(swap16_add(dpp_add<0x128>(a[k])));
        float* red = (float*)lds;
        if (lane < 8) { *(f32x4*)&red[w * 64 + lane * 8] = (f32x4){a[0], a[1], a[2], a[3]}; *(f32x4*)&red[w * 64 + lane * 8 + 4] = (f32x4){a[4], a[5], a[6], a[7]}; }
        BARRIER();
        if (tid < 32) {
            f32x2 c = {0.f, 0.f};
#pragma unroll
            for (int i = 0; i < 8; ++i) c += *(const f32x2*)&red[i * 64 + 2 * tid];
            st16_sc1(P.ws + WS_GCS, (unsigned)(sl * 1536 + cgp * 64 + 2 * tid) * 8, (u32x4){__float_as_uint(c[0]), tg, __float_as_uint(c[1]), tg});
        }
    }
    STAMP(6);
    if constexpr (FUSED) publish(P.ws, FL_S + j, tag); else { WAITV(0); BARRIER(); }
    STAMP(7);
}

template <int MASK, bool FUSED>
__global__ __launch_bounds__(512, 4) void mega(Params P) {
    extern __shared__ __attribute__((aligned(16))) char lds[];
    u64 tag = 0;
    if constexpr (FUSED) {
        tag = flag_load(P.ws, FL_GEN) + 1;
    }
    STAMP((MASK & 1) ? 0 : (MASK & 2) ? 11 : (MASK & 8) ? 14 : 12);
    if (blockIdx.x < 256) {
        if constexpr (MASK & 1) phase_q<FUSED>(P, lds, tag);
        if constexpr (MASK & 2) phase_u<FUSED>(P, lds, tag);
        if constexpr (MASK & 4) { BARRIER(); phase_d<FUSED>(P, lds, tag); }
    } else {
        if constexpr (MASK & 1) phase_side<FUSED>(P, lds, tag);
        if constexpr (MASK & 8) phase_attn<FUSED>(P, lds, tag);
    }
}

extern "C" void kernel_launch(void* const* d_in, const int* in_sizes, int n_in, void* d_out, int out_size, void* d_ws, size_t ws_size, hipStream_t stream) {
    Params P;
    P.x = (const char*)d_in[0]; P.wqkv = (const char*)d_in[1]; P.wo = (const char*)d_in[2];
    P.wg = (const char*)d_in[4]; P.wu = (const char*)d_in[5]; P.wd = (const char*)d_in[6];
    P.out = (char*)d_out; P.ws = (char*)d_ws;
    (void)hipFuncSetAttribute((const void*)mega<15, true>, hipFuncAttributeMaxDynamicSharedMemorySize, LDS_BYTES);
    mega<15, true><<<512, 512, LDS_BYTES, stream>>>(P);
}
```

```cpp
#define NO_PF
#define WPF 1
#define APRIO 1
#include <hip/hip_runtime.h>
#include <hip/hip_bf16.h>
#include <stdint.h>

typedef short bf16x8 __attribute__((ext_vector_type(8)));
typedef short s16x4 __attribute__((ext_vector_type(4)));
typedef short v4i16_t __attribute__((ext_vector_type(4)));
typedef float f32x16 __attribute__((ext_vector_type(16)));
typedef float f32x4 __attribute__((ext_vector_type(4)));
typedef float f32x2 __attribute__((ext_vector_type(2)));
typedef unsigned u32x4 __attribute__((ext_vector_type(4)));
typedef unsigned u32x2 __attribute__((ext_vector_type(2)));
typedef __bf16 bf2_t __attribute__((ext_vector_type(2)));
typedef unsigned long long u64;
#define LDSP __attribute__((address_space(3)))
#define GLBP __attribute__((address_space(1)))
typedef LDSP const char* lds_cptr;
typedef LDSP char* lds_ptr;

constexpr int SEQ = 2048, DM = 512, DF = 1792, NH = 32;
constexpr size_t WS_Q = 0, WS_K = 2u << 20, WS_V = 4u << 20, WS_O = 6u << 20, WS_H = 8u << 20, WS_STATS = 16u << 20, WS_FLAGS = (16u << 20) + 131072;
constexpr int FL_GEN = 0, FL_START = 16, FL_Q = 16 + 512, FL_H = FL_Q + 256, FL_O = FL_H + 512, FL_S = FL_O + 256, FL_QL = FL_S + 256, FL_END = FL_QL + 256;
constexpr size_t WS_GST = (16u << 20), WS_GCS = (16u << 20) + 32768;
constexpr size_t WS_XN = 18u << 20;
constexpr float QSCALE = 0.25f * 1.4426950408889634f;
constexpr int LDS_BYTES = 81920;

#define MFMA32(a, b, c) __builtin_amdgcn_mfma_f32_32x32x16_bf16((a), (b), (c), 0, 0, 0)
#define SBAR() __builtin_amdgcn_sched_barrier(0)
#define PIN(x) asm volatile("" : "+v"(x))

__device__ __forceinline__ unsigned cvtpk(float lo, float hi) { return __builtin_bit_cast(unsigned, __builtin_convertvector(((f32x2){lo, hi}), bf2_t)); }
__device__ __forceinline__ unsigned uni(unsigned v) { return (unsigned)__builtin_amdgcn_readfirstlane((int)v); }
template <class T> __device__ __forceinline__ T* uni(T* p) { const u64 v = (u64)p; return (T*)(((u64)uni((unsigned)(v >> 32)) << 32) | uni((unsigned)v)); }
#define GLDS_ASM(SC) \
    if constexpr (NP == 1) asm volatile("s_mov_b32 m0, %2\n\ts_nop 0\n\tglobal_load_lds_dwordx4 %0, %1" SC :: "v"(v0), "s"(gbase), "s"(lds_base) : "memory"); \
    else if constexpr (NP == 2) asm volatile("s_mov_b32 m0, %3\n\ts_nop 0\n\tglobal_load_lds_dwordx4 %0, %2" SC "\n\tglobal_load_lds_dwordx4 %1, %2 offset:1024" SC :: "v"(v0), "v"(v1), "s"(gbase), "s"(lds_base) : "memory"); \
    else if constexpr (NP == 3) asm volatile("s_mov_b32 m0, %4\n\ts_nop 0\n\tglobal_load_lds_dwordx4 %0, %3" SC "\n\tglobal_load_lds_dwordx4 %1, %3 offset:1024" SC "\n\tglobal_load_lds_dwordx4 %2, %3 offset:2048" SC :: "v"(v0), "v"(v1), "v"(v2), "s"(gbase), "s"(lds_base) : "memory"); \
    else asm volatile("s_mov_b32 m0, %5\n\ts_nop 0\n\tglobal_load_lds_dwordx4 %0, %4" SC "\n\tglobal_load_lds_dwordx4 %1, %4 offset:1024" SC "\n\tglobal_load_lds_dwordx4 %2, %4 offset:2048" SC "\n\tglobal_load_lds_dwordx4 %3, %4 offset:3072" SC :: "v"(v0), "v"(v1), "v"(v2), "v"(v3), "s"(gbase), "s"(lds_base) : "memory");
template <bool SC1, int NP>
__device__ __forceinline__ void gldsN(const void* gbase, unsigned lds_base, unsigned v0, unsigned v1 = 0, unsigned v2 = 0, unsigned v3 = 0) {
    if constexpr (SC1) { GLDS_ASM(" sc1") } else { GLDS_ASM("") }
}
template <bool SC1>
__device__ __forceinline__ void glds16(const void* gbase, unsigned voff, unsigned lds_base) { gldsN<SC1, 1>(gbase, lds_base, voff); }
__device__ __forceinline__ void st16_sc1(void* gbase, unsigned voff, u32x4 v) { asm volatile("s_nop 4\n\tglobal_store_dwordx4 %0, %1, %2 sc1\n\ts_nop 1" :: "v"(voff), "v"(v), "s"(uni(gbase)) : "memory"); }
__device__ __forceinline__ void st8_sc1(void* gbase, unsigned voff, f32x2 v) { asm volatile("s_nop 4\n\tglobal_store_dwordx2 %0, %1, %2 sc1\n\ts_nop 1" :: "v"(voff), "v"(v), "s"(uni(gbase)) : "memory"); }
__device__ __forceinline__ bf16x8 ldsr128(lds_cptr p) { return *(const LDSP bf16x8*)p; }
__device__ __forceinline__ s16x4 vtr(lds_cptr p) { return __builtin_bit_cast(s16x4, __builtin_amdgcn_ds_read_tr16_b64_v4i16((LDSP v4i16_t*)p)); }
__device__ __forceinline__ bf16x8 cat8(s16x4 a, s16x4 b) { return (bf16x8){a[0], a[1], a[2], a[3], b[0], b[1], b[2], b[3]}; }
__device__ __forceinline__ float dot2(unsigned a, unsigned b, float c) { return __builtin_amdgcn_fdot2_f32_bf16(__builtin_bit_cast(bf2_t, a), __builtin_bit_cast(bf2_t, b), c, false); }
__device__ __forceinline__ float swap_add(float v) {
    auto rr = __builtin_amdgcn_permlane32_swap(__float_as_uint(v), __float_as_uint(v), false, false);
    return __uint_as_float(rr[0]) + __uint_as_float(rr[1]);
}
__device__ __forceinline__ float swap_max(float v) {
    auto rr = __builtin_amdgcn_permlane32_swap(__float_as_uint(v), __float_as_uint(v), false, false);
    return __builtin_fmaxf(__uint_as_float(rr[0]), __uint_as_float(rr[1]));
}
template <int CTRL> __device__ __forceinline__ float dpp_add(float v) { return v + __int_as_float(__builtin_amdgcn_update_dpp(0, __float_as_int(v), CTRL, 0xf, 0xf, true)); }
__device__ __forceinline__ float swap16_add(float v) {
    auto rr = __builtin_amdgcn_permlane16_swap(__float_as_uint(v), __float_as_uint(v), false, false);
    return __uint_as_float(rr[0]) + __uint_as_float(rr[1]);
}
__device__ __forceinline__ float wave_sum(float v) {
    v = dpp_add<0xB1>(v); v = dpp_add<0x4E>(v); v = dpp_add<0x141>(v); v = dpp_add<0x140>(v);
    return swap_add(swap16_add(v));
}
#define WAITV(n) asm volatile("s_waitcnt vmcnt(" #n ")" ::: "memory")
#define BARRIER() asm volatile("s_waitcnt lgkmcnt(0)\n\ts_barrier" ::: "memory")
__device__ __forceinline__ float bflo(unsigned u) { return __uint_as_float(u << 16); }
__device__ __forceinline__ float bfhi(unsigned u) { return __uint_as_float(u & 0xffff0000u); }

#define STAMP(slot) do {} while (0)

struct Params { const char* x; const char* wqkv; const char* wo; const char* wg; const char* wu; const char* wd; char* out; char* ws; };

__device__ __forceinline__ u64 flag_load(const char* ws, int idx) { return __hip_atomic_load((const GLBP u64*)(ws + WS_FLAGS) + idx, __ATOMIC_RELAXED, __HIP_MEMORY_SCOPE_AGENT); }
__device__ __forceinline__ void flag_store(char* ws, int idx, u64 v) { __hip_atomic_store((GLBP u64*)(ws + WS_FLAGS) + idx, v, __ATOMIC_RELAXED, __HIP_MEMORY_SCOPE_AGENT); }
__device__ __forceinline__ void publish(char* ws, int idx, u64 tag) {
    WAITV(0); BARRIER();
    if (threadIdx.x == 0) flag_store(ws, idx, tag);
}
template <bool WAIT>
__device__ __forceinline__ void wait_flags(char* ws, int idx, int n, u64 tag) {
    if constexpr (WAIT) {
        if (threadIdx.x < 64) {
            const int i = idx + ((int)threadIdx.x < n ? (int)threadIdx.x : 0);
            for (unsigned spin = 0; spin < (1u << 22); ++spin) {
                const u64 v = flag_load(ws, i);
                if (__all(v == tag)) break;
                __builtin_amdgcn_s_sleep(2);
            }
        }
    }
    BARRIER();
}

__device__ __forceinline__ void waitv_n(int n) {
    if (n <= 0) WAITV(0); else if (n == 1) WAITV(1); else if (n == 2) WAITV(2); else if (n == 3) WAITV(3); else if (n == 4) WAITV(4); else if (n == 5) WAITV(5);
    else if (n == 6) WAITV(6); else if (n == 7) WAITV(7); else if (n == 8) WAITV(8); else if (n == 9) WAITV(9); else WAITV(10);
}
#define SGB(mask, n) __builtin_amdgcn_sched_group_barrier(mask, n, 0)
template <int NM, int NR, int NV>
__device__ __forceinline__ void interleave() {
    if constexpr (NV < 0) {
#pragma unroll
        for (int i = 0; i < NM; ++i) { SGB(0x8, 1); if (3 * i < NR) SGB(0x100, 3); }
    } else {
#pragma unroll
        for (int i = 0; i < NM; ++i) { SGB(0x8, 1); SGB(0x100, (NR + NM - 1) / NM); if (NV) SGB(0x2, (NV + NM - 1) / NM); }
    }
}
template <int N, int D, int NPW, class Issue> __device__ __forceinline__ void loader_loop(Issue issue, u64* dbg = nullptr);
#define TS(t) asm volatile("s_memtime %0\n\ts_waitcnt lgkmcnt(0)" : "=s"(t) :: "memory")
template <int D, int NPW, int SB, int NM, int NR, int NV, class Frag, class Issue, class Read, class Mma, class After>
__device__ __forceinline__ void gemm_loop_rt(const int N, Issue issue, Read read, Mma mma, After after) {
    Frag f0, f1;
    if constexpr (NPW > 0) waitv_n((D - 1) * NPW);
    BARRIER();
    read(f0, 0, 0, 0);
    int rs = 0;
#pragma nounroll
    for (int s = 0; s < N - D; ++s) {
        const int ns = rs == D - 1 ? 0 : rs + 1;
        read(f1, rs * SB, 1, s); mma(f0, 0); interleave<NM, NR, NV>(); SBAR();
        if constexpr (NPW > 0) waitv_n((D - 2) * NPW);
        BARRIER();
        issue(s + D, rs); SBAR();
        read(f0, ns * SB, 0, s + 1); mma(f1, 1); interleave<NM, NR, NV>(); SBAR();
        after(s);
        rs = ns;
    }
#pragma unroll
    for (int t = 0; t < D; ++t) {
        const int ns = rs == D - 1 ? 0 : rs + 1;
        read(f1, rs * SB, 1, N - D + t); mma(f0, 0); interleave<NM, NR, NV>(); SBAR();
        if (t < D - 1) { if constexpr (NPW > 0) waitv_n((D - 2 - t) * NPW); BARRIER(); read(f0, ns * SB, 0, N - D + t + 1); }
        mma(f1, 1); if (t < D - 1) interleave<NM, NR, NV>(); SBAR();
        rs = ns;
    }
}
template <int D, int NPW, class Issue>
__device__ __forceinline__ void loader_loop_rt(const int N, Issue issue) {
    waitv_n((D - 1) * NPW); BARRIER();
    int rs = 0;
#pragma nounroll
    for (int s = 0; s < N - D; ++s) {
        waitv_n((D - 2) * NPW); BARRIER();
        issue(s + D, rs);
        rs = rs == D - 1 ? 0 : rs + 1;
    }
#pragma unroll
    for (int t = 0; t < D - 1; ++t) { waitv_n((D - 2 - t) * NPW); BARRIER(); }
}
template <int N, int D, int NPW, int SB, int NM, int NR, int NV, class Frag, class Issue, class Read, class Mma>
__device__ __forceinline__ void gemm_loop(Issue issue, Read read, Mma mma, u64* dbg = nullptr) {
    Frag f0, f1;
    waitv_n((D - 1) * NPW); BARRIER();
    read(f0, 0, 0, 0);
    int rs = 0;
#pragma nounroll
    for (int s = 0; s < N - D; ++s) {
        const int ns = rs == D - 1 ? 0 : rs + 1;
        read(f1, rs * SB, 1, s); mma(f0, 0); interleave<NM, NR, NV>(); SBAR();
        waitv_n((D - 2) * NPW); BARRIER();
        issue(s + D, rs); SBAR();
        read(f0, ns * SB, 0, s + 1); mma(f1, 1); interleave<NM, NR, NV>(); SBAR();
        rs = ns;
    }
#pragma unroll
    for (int t = 0; t < D; ++t) {
        const int ns = rs == D - 1 ? 0 : rs + 1;
        read(f1, rs * SB, 1, N - D + t); mma(f0, 0); interleave<NM, NR, NV>(); SBAR();
        if (t < D - 1) { waitv_n((D - 2 - t) * NPW); BARRIER(); read(f0, ns * SB, 0, N - D + t + 1); }
        mma(f1, 1); if (t < D - 1) interleave<NM, NR, NV>(); SBAR();
        rs = ns;
    }
}
template <int N, int D, int SB, int NM, int NR, int NV, class Frag, class Read, class Mma>
__device__ __forceinline__ void cons_loop(Read read, Mma mma, char* wsp = nullptr, int slot0 = 0) {
    static_assert(N % 2 == 0, "");
    Frag fa, fb;
    BARRIER();
    read(fa, 0, 0);
    int ns = 1;
#pragma nounroll
    for (int s = 0; s < N - 2; s += 2) {
        BARRIER();
        read(fb, ns * SB, s + 1); mma(fa); interleave<NM, NR, NV>(); SBAR();
        ns = ns == D - 1 ? 0 : ns + 1;
        BARRIER();
        read(fa, ns * SB, s + 2); mma(fb); interleave<NM, NR, NV>(); SBAR();
        ns = ns == D - 1 ? 0 : ns + 1;
    }
    BARRIER();
    read(fb, ns * SB, N - 1); mma(fa); interleave<NM, NR, NV>(); SBAR();
    mma(fb); SBAR();
}
template <int N, int D, int NPW, class Issue>
__device__ __forceinline__ void loader_loop(Issue issue, u64* dbg) {
    waitv_n((D - 1) * NPW); BARRIER();
    int rs = 0;
#pragma nounroll
    for (int s = 0; s < N - D; ++s) {
        waitv_n((D - 2) * NPW);
        BARRIER();
        issue(s + D, rs);
        rs = rs == D - 1 ? 0 : rs + 1;
    }
#pragma unroll
    for (int t = 0; t < D - 1; ++t) { waitv_n((D - 2 - t) * NPW); BARRIER(); }
}
struct Frag2 { bf16x8 a, b; };
struct Frag3 { bf16x8 a, b, c; };
struct Frag4 { bf16x8 a, b, c, d; };
__device__ __forceinline__ float sum2x4(bf16x8 v, float acc) { const u32x4 u = __builtin_bit_cast(u32x4, v);
    acc = dot2(u[0], 0x3f803f80u, acc); acc = dot2(u[1], 0x3f803f80u, acc); acc = dot2(u[2], 0x3f803f80u, acc); return dot2(u[3], 0x3f803f80u, acc); }
__device__ __forceinline__ float sq2x4(bf16x8 v, float acc) { const u32x4 u = __builtin_bit_cast(u32x4, v);
    acc = dot2(u[0], u[0], acc); acc = dot2(u[1], u[1], acc); acc = dot2(u[2], u[2], acc); return dot2(u[3], u[3], acc); }

constexpr int Q_ST = 4, Q_WB = 12288, Q_A0 = Q_ST * Q_WB, Q_ACH = 8192, Q_SC = Q_A0 + 3 * Q_ACH;
template <bool FUSED>
__device__ __forceinline__ void phase_q(const Params& P, char* lds, u64 tag) {
    const int tid = threadIdx.x, lane = tid & 63, r = lane & 31, hh = lane >> 5;
    const int w = __builtin_amdgcn_readfirstlane(tid >> 6);
    const int b = blockIdx.x & 255, xcd = b & 7, bi = b >> 3;
    const int ri = bi & 7, cj = bi >> 3;
    const int rb = 8 * (2 * (xcd & 1) + ((xcd >> 1) & 1)) + ri, cg = 4 * (xcd >> 2) + cj;
    const int kc0 = 0;
    const int m0 = rb * 64;
    const unsigned lds0 = (unsigned)(uintptr_t)lds;
    float* sc = (float*)(lds + Q_SC);
    if (w >= 4) {
        const int p = w - 4;
        unsigned woff[3];
        unsigned aoff0;
        { const int row = 16 * p + (lane >> 3), c = (lane & 7) ^ ((row >> 1) & 7); aoff0 = row * 1024 + c * 16; }
#pragma unroll
        for (int i = 0; i < 3; ++i) { const int pc = 64 * (3 * p + i) + lane, row = pc / 24, ch = pc % 24, cs = (ch & 7) ^ (((row >> 1) & 1) << 2); woff[i] = row * 3072 + (ch >> 3) * 1024 + cg * 128 + cs * 16; }
        woff[1] -= 1024; woff[2] -= 2048;
        const char* ag = uni(P.x + (size_t)m0 * 1024);
        const char* wq = uni(P.wqkv);
        const unsigned ldsW = uni(lds0 + 3 * p * 1024), ldsX = uni(lds0 + Q_A0 + 2 * p * 1024);
        auto issueW = [&](int st, int slot) { const int kt = (2 * kc0 + st) & 15; gldsN<false, 3>(wq + kt * (32 * 3072), ldsW + slot * Q_WB, woff[0], woff[1], woff[2]); };
        auto issueX = [&](int c, int cslot, int set) { const int kc = (kc0 + c) & 7; glds16<false>(ag + kc * 128 + set * 8192, aoff0 ^ (unsigned)(set << 6), ldsX + cslot * Q_ACH + set * 1024); };
        issueX(0, 0, 0); issueX(0, 0, 1); issueW(0, 0); issueX(1, 1, 0); issueX(1, 1, 1); issueW(1, 1); issueW(2, 2); issueX(2, 2, 0); issueX(2, 2, 1); issueW(3, 3);
        loader_loop<16, Q_ST, 4>([&](int st, int slot) {
            const int u = st + 1; const int c = (u >> 1) < 7 ? (u >> 1) : 7;
            issueW(st, slot); issueX(c, c % 3, u & 1); }, (u64*)(P.ws + (40u << 20)) + (size_t)(blockIdx.x * 8 + w) * 64);
        if constexpr (FUSED) { if (w == 4 && lane == 0) flag_store(P.ws, FL_QL + b, tag); }
        {
            const unsigned tg = (unsigned)tag;
            if (p == 0) {
                u32x4 v;
                for (unsigned spin = 0; spin < (1u << 16); ++spin) {
                    asm volatile("s_nop 4\n\tglobal_load_dwordx4 %0, %1, %2 sc1\n\ts_waitcnt vmcnt(0)" : "=v"(v) : "v"((unsigned)(m0 + lane) * 16), "s"(uni(P.ws + WS_GST)) : "memory");
                    if (__all(v[1] == tg && v[3] == tg)) break;
                    __builtin_amdgcn_s_sleep(1);
                }
                *(f32x2*)&sc[lane * 2] = (f32x2){__uint_as_float(v[0]), __uint_as_float(v[2])};
            } else {
                u32x4 v0, v1;
                const unsigned off = (unsigned)(((lane >> 5) * 2) * 1536 + (p - 1) * 512 + cg * 64 + 2 * (lane & 31)) * 8;
                for (unsigned spin = 0; spin < (1u << 16); ++spin) {
                    asm volatile("s_nop 4\n\tglobal_load_dwordx4 %0, %2, %3 sc1\n\tglobal_load_dwordx4 %1, %4, %3 sc1\n\ts_waitcnt vmcnt(0)" : "=&v"(v0), "=&v"(v1) : "v"(off), "s"(uni(P.ws + WS_GCS)), "v"(off + 1536 * 8) : "memory");
                    if (__all(v0[1] == tg && v0[3] == tg && v1[1] == tg && v1[3] == tg)) break;
                    __builtin_amdgcn_s_sleep(1);
                }
                const float c0 = swap_add(__uint_as_float(v0[0]) + __uint_as_float(v1[0])), c1 = swap_add(__uint_as_float(v0[2]) + __uint_as_float(v1[2]));
                if (lane < 32) *(f32x2*)&sc[128 + (p - 1) * 64 + 2 * lane] = (f32x2){c0, c1};
            }
        }
        BARRIER();
        STAMP(2);
    } else {
        const int rt = w >> 1, hf = w & 1;
        const int arow = 32 * rt + r;
        unsigned a_rd[2];
#pragma unroll
        for (int k = 0; k < 2; ++k) a_rd[k] = Q_A0 + arow * 128 + ((2 * k + hh) ^ ((arow >> 1) & 7)) * 16;
        const int q4 = (lane & 15) >> 2;
        const unsigned chw = (4 * hf + 2 * ((lane >> 4) & 1) + ((lane & 3) >> 1)) ^ (((q4 >> 1) & 1) << 2);
        const unsigned w_rd = (8 * hh + q4) * 384 + chw * 16 + 8 * (lane & 1);
        unsigned pf0;
        {
            const int idx = w * 64 + lane;
            const char* a;
            if (idx < 144) { const int st = 4 + idx / 12, l = ri * 12 + idx % 12; a = P.wqkv + (size_t)(st * 32 + l / 3) * 3072 + (l % 3) * 1024 + cg * 128; }
            else { const int i2 = idx < 224 ? idx - 144 : 0; a = P.x + (size_t)(m0 + cj * 16 + (i2 & 15)) * 1024 + (3 + (i2 >> 4)) * 128; }
            pf0 = 0;
        }
        f32x16 accQ = {}, accK = {}, accV = {};
        struct FragQ { Frag4 k0, k1; };
        auto rd = [&](Frag4& f, lds_cptr xa, lds_cptr wb) { f.a = ldsr128(xa);
            f.b = cat8(vtr(wb), vtr(wb + 1536)); f.c = cat8(vtr(wb + 128), vtr(wb + 128 + 1536)); f.d = cat8(vtr(wb + 256), vtr(wb + 256 + 1536)); };
        cons_loop<16, Q_ST, Q_WB, 6, 14, 0, FragQ>(
            [&](FragQ& f, int so, int stg) { lds_cptr sb = (lds_cptr)lds + so + w_rd; lds_cptr xb = (lds_cptr)lds + ((stg >> 1) % 3) * Q_ACH; const unsigned fl = (stg & 1) << 6;
                rd(f.k0, xb + (a_rd[0] ^ fl), sb); rd(f.k1, xb + (a_rd[1] ^ fl), sb + 6144); },
            [&](const FragQ& f) {
                accQ = MFMA32(f.k0.b, f.k0.a, accQ); accK = MFMA32(f.k0.c, f.k0.a, accK); accV = MFMA32(f.k0.a, f.k0.d, accV);
                accQ = MFMA32(f.k1.b, f.k1.a, accQ); accK = MFMA32(f.k1.c, f.k1.a, accK); accV = MFMA32(f.k1.a, f.k1.d, accV);
                PIN(accQ); PIN(accK); PIN(accV); }, P.ws, 1);
        STAMP(13);
        asm volatile("s_waitcnt vmcnt(0)" : "+v"(pf0) :: "memory");
        BARRIER(); SBAR();
        STAMP(2);
        const int tile = rb * 2 + rt, hd0 = cg * 4 + hf * 2;
        {
            const f32x2 st = *(const f32x2*)&sc[arow * 2];
            const float mu = st[0], rstd = st[1];
#pragma unroll
            for (int isk = 0; isk < 2; ++isk) {
                const f32x16& acc = isk ? accK : accQ;
                const float scl = isk ? rstd : rstd * QSCALE;
                unsigned pk[8];
#pragma unroll
                for (int q = 0; q < 4; ++q) {
                    const f32x4 c4 = *(const f32x4*)&sc[128 + isk * 64 + hf * 32 + 8 * q + 4 * hh];
                    const float v0 = (acc[4 * q] - mu * c4[0]) * scl, v1 = (acc[4 * q + 1] - mu * c4[1]) * scl, v2 = (acc[4 * q + 2] - mu * c4[2]) * scl, v3 = (acc[4 * q + 3] - mu * c4[3]) * scl;
                    pk[2 * q] = cvtpk(v0, v1); pk[2 * q + 1] = cvtpk(v2, v3); }
                char* dst = P.ws + (isk ? WS_K : WS_Q);
#pragma unroll
                for (int hd = 0; hd < 2; ++hd) st16_sc1(dst, (unsigned)(((hd0 + hd) * 64 + tile) * 64 + lane) * 16, (u32x4){pk[4 * hd], pk[4 * hd + 1], pk[4 * hd + 2], pk[4 * hd + 3]});
                SBAR();
            }
        }
        {
            const float csvv = sc[128 + 128 + hf * 32 + r];
            unsigned pk[8];
#pragma unroll
            for (int q = 0; q < 4; ++q) { const f32x4 s0 = *(const f32x4*)&sc[(32 * rt + 8 * q + 4 * hh) * 2], s1 = *(const f32x4*)&sc[(32 * rt + 8 * q + 4 * hh) * 2 + 4];
                const float v0 = (accV[4 * q] - s0[0] * csvv) * s0[1], v1 = (accV[4 * q + 1] - s0[2] * csvv) * s0[3], v2 = (accV[4 * q + 2] - s1[0] * csvv) * s1[1], v3 = (accV[4 * q + 3] - s1[2] * csvv) * s1[3];
                pk[2 * q] = cvtpk(v0, v1); pk[2 * q + 1] = cvtpk(v2, v3); }
            const int head = hd0 + (r >> 4), d = r & 15;
#pragma unroll
            for (int s = 0; s < 2; ++s) st16_sc1(P.ws + WS_V, (unsigned)((((head * 128 + tile * 2 + s) * 2 + hh) * 16 + d) * 16), (u32x4){pk[4 * s], pk[4 * s + 1], pk[4 * s + 2], pk[4 * s + 3]});
        }
    }
    if constexpr (FUSED) { publish(P.ws, FL_Q + cg * 32 + rb, tag); if (threadIdx.x == 0) flag_store(P.ws, FL_START + blockIdx.x, tag); } else { WAITV(0); BARRIER(); }
    STAMP(3);
}

constexpr int G_ST = 5, G_AB = 8192, G_SB = 16384;
template <bool FUSED>
__device__ __forceinline__ void phase_u(const Params& P, char* lds, u64 tag) {
    const int tid = threadIdx.x, lane = tid & 63, r = lane & 31, hh = lane >> 5;
    const int w = __builtin_amdgcn_readfirstlane(tid >> 6), wm = (w >> 1) & 1, wn = w & 1;
    const int b = blockIdx.x & 255, xcd = b & 7, bi = b >> 3;
    const bool isA = blockIdx.x >= 256;
    if (!isA && bi >= 24) return;
    constexpr int ntile = 1;
    const int rb = 8 * (xcd & 1) + (bi < 24 ? 2 * (bi & 3) + (isA ? 0 : 1) : bi - 24), cb0 = 7 * (xcd >> 1) + (bi < 24 ? bi >> 2 : 6);
    const int m0 = rb * 128;
    const unsigned lds0 = (unsigned)(uintptr_t)lds;
    unsigned pf = 0;
    if (isA && w >= 4) {
        const int gw = bi * 4 + (w & 3), li = (gw < 112 ? gw : 0) * 64 + lane, m = li / 3584, l2 = li % 3584, row = l2 / 7, c = l2 % 7;
        const char* a = (m ? P.wu : P.wg) + (size_t)row * (DF * 2) + ((xcd >> 1) * 7 + c) * 128;
        asm volatile("global_load_dword %0, %1, off" : "=v"(pf) : "v"(a) : "memory");
    }
    wait_flags<FUSED>(P.ws, FL_S + 16 * rb, 16, tag);
    STAMP(isA ? 8 : 4);
    if (w >= 4) {
        const int p = w & 3;
        unsigned woff;
        { const int row = 8 * p + (lane >> 3), ch = (lane & 7) ^ (((row >> 1) & 1) << 2); woff = row * (DF * 2) + cb0 * 128 + ch * 16; }
        const char* ag = uni(P.ws + WS_XN + (size_t)m0 * 64 + p * 2048);
        const char* wgp = uni(P.wg); const char* wup = uni(P.wu);
        const unsigned ldsA = uni(lds0 + 2 * p * 1024), ldsW = uni(lds0 + G_AB + p * 1024);
        auto issue = [&](int st, int slot) { const int kt = st & 15; const int wo = kt * (32 * DF * 2) + (st >> 4) * 512;
            gldsN<true, 2>(ag + kt * (2048 * 64), ldsA + slot * G_SB, lane * 16, lane * 16);
            glds16<false>(wgp + wo, woff, ldsW + slot * G_SB); glds16<false>(wup + wo, woff, ldsW + 4096 + slot * G_SB); };
#pragma unroll
        for (int i = 0; i < G_ST; ++i) issue(i, i);
        loader_loop_rt<G_ST, 4>(16 * ntile, issue);
        asm volatile("s_waitcnt vmcnt(0)" : "+v"(pf) :: "memory");
    } else {
        f32x16 acc[2][2] = {};
        unsigned a_rd[2];
        { const int arow = 64 * wm + r; a_rd[0] = arow * 64 + ((0 + hh) ^ ((arow >> 2) & 3)) * 16; a_rd[1] = arow * 64 + ((2 + hh) ^ ((arow >> 2) & 3)) * 16; }
        const int q4 = (lane & 15) >> 2;
        const unsigned chg = (4 * wn + 2 * ((lane >> 4) & 1) + ((lane & 3) >> 1)) ^ (((q4 >> 1) & 1) << 2);
        const unsigned w_rd = G_AB + (8 * hh + q4) * 128 + chg * 16 + 8 * (lane & 1);
        auto epilogue = [&](int cb) {
            constexpr float GA = -2.f * 0.7978845608028654f * 1.4426950408889634f, GB = GA * 0.044715f;
#pragma unroll
            for (int i = 0; i < 2; ++i) {
                unsigned pk[8];
#pragma unroll
                for (int q = 0; q < 4; ++q) {
                    float hv[4];
#pragma unroll
                    for (int j = 0; j < 4; ++j) {
                        const float gv = acc[i][0][4 * q + j], uv = acc[i][1][4 * q + j];
                        const float z = gv * (GA + GB * gv * gv);
                        hv[j] = gv * uv * __builtin_amdgcn_rcpf(1.f + __builtin_amdgcn_exp2f(z));
                    }
                    pk[2 * q] = cvtpk(hv[0], hv[1]); pk[2 * q + 1] = cvtpk(hv[2], hv[3]);
                }
                const unsigned off = (unsigned)(((rb * 4 + 2 * wm + i) * 112 + cb * 4 + 2 * wn) * 64 + lane) * 16;
                st16_sc1(P.ws + WS_H, off, (u32x4){pk[0], pk[1], pk[2], pk[3]}); st16_sc1(P.ws + WS_H, off + 1024, (u32x4){pk[4], pk[5], pk[6], pk[7]});
            } };
        gemm_loop_rt<G_ST, 0, G_SB, 4, 6, -1, Frag4>(16 * ntile, [&](int, int) {},
            [&](Frag4& f, int so, int kk, int) { lds_cptr sb = (lds_cptr)lds + so;
                f.c = ldsr128(sb + a_rd[kk]);
                f.a = cat8(vtr(sb + w_rd + kk * 2048), vtr(sb + w_rd + kk * 2048 + 512));
                f.b = cat8(vtr(sb + w_rd + 4096 + kk * 2048), vtr(sb + w_rd + 4096 + kk * 2048 + 512));
                f.d = ldsr128(sb + a_rd[kk] + 2048); },
            [&](const Frag4& f, int) {
                acc[0][0] = MFMA32(f.a, f.c, acc[0][0]); acc[0][1] = MFMA32(f.b, f.c, acc[0][1]);
                acc[1][0] = MFMA32(f.a, f.d, acc[1][0]); acc[1][1] = MFMA32(f.b, f.d, acc[1][1]); PIN(acc[0][0]); PIN(acc[0][1]); PIN(acc[1][0]); PIN(acc[1][1]); },
            [&](int s) { if (s == 15) { epilogue(cb0); acc[0][0] = (f32x16){}; acc[0][1] = (f32x16){}; acc[1][0] = (f32x16){}; acc[1][1] = (f32x16){}; SBAR(); } });
        STAMP(isA ? 9 : 5);
        epilogue(cb0 + 4 * (ntile - 1));
    }
    if constexpr (FUSED) {
        WAITV(0); BARRIER();
        if (threadIdx.x == 0) flag_store(P.ws, FL_H + rb * 32 + cb0, tag);
    } else { WAITV(0); BARRIER(); }
    STAMP(isA ? 10 : 6);
}

constexpr int D_ST = 4, D_AB = 8192, D_SB = 16384;
template <bool FUSED>
__device__ __forceinline__ void phase_d(const Params& P, char* lds, u64 tag) {
    const int tid = threadIdx.x, lane = tid & 63, r = lane & 31, hh = lane >> 5;
    const int w = __builtin_amdgcn_readfirstlane(tid >> 6), wk = w >> 2, wm = (w >> 1) & 1, wn = w & 1;
    const int b = blockIdx.x & 255, xcd = b & 7, bi = b >> 3, rb = xcd * 4 + (bi & 3), cb = bi >> 2;
    const int m0 = rb * 64, n0 = cb * 64;
    const int kt0 = 0;
    const unsigned lds0 = (unsigned)(uintptr_t)lds;
    constexpr int NT = 28;
    const int row = m0 + 32 * wm + r;
    u32x2 xv[2];
#pragma unroll
    for (int q = 0; q < 2; ++q) xv[q] = *(const u32x2*)(P.x + (size_t)row * 1024 + (n0 + 32 * wn + 4 * hh + 8 * (2 * wk + q)) * 2);
    const int q4 = (lane & 15) >> 2;
    const unsigned chw = (4 * wn + 2 * ((lane >> 4) & 1) + ((lane & 3) >> 1)) ^ (((q4 >> 1) & 1) << 2);
    const unsigned w_rd = (4 * hh + q4) * 128 + chw * 16 + 8 * (lane & 1);
    f32x16 acc = {};
    if (wk == 1) {
        const int p = w & 3;
        unsigned aoff[2], woff2[2];
#pragma unroll
        for (int i = 0; i < 2; ++i) { const int rw = 8 * (2 * p + i) + (lane >> 3); aoff[i] = lane * 16;
            const int ch = (lane & 7) ^ (((rw >> 1) & 1) << 2); woff2[i] = rw * 1024 + n0 * 2 + ch * 16 - 1024 * i; }
        const char* hg = uni(P.ws + WS_H + (size_t)((rb * 2 + (p >> 1)) * 112 + 2 * (p & 1)) * 1024); const char* wd = uni(P.wd);
        const unsigned ldsA = uni(lds0 + 2 * p * 1024), ldsW = uni(lds0 + D_AB + 2 * p * 1024);
        auto issue_w = [&](int it, int slot) { int kt = kt0 + it; if (kt >= NT) kt -= NT; gldsN<false, 2>(wd + kt * (64 * 1024), ldsW + slot * D_SB, woff2[0], woff2[1]); };
        auto issue_a = [&](int it, int slot) { int kt = kt0 + it; if (kt >= NT) kt -= NT; gldsN<true, 2>(hg + kt * 4096, ldsA + slot * D_SB, aoff[0], aoff[1]); };
#pragma unroll
        for (int i = 0; i < D_ST; ++i) issue_w(i, i);
        wait_flags<FUSED>(P.ws, FL_H + (rb >> 1) * 32, 28, tag);
#pragma unroll
        for (int i = 0; i < D_ST; ++i) { issue_a(i, i); issue_w(i, i); }
        loader_loop<NT, D_ST, 4>([&](int it, int slot) { issue_a(it, slot); issue_w(it, slot); });
    } else {
        wait_flags<FUSED>(P.ws, FL_H + (rb >> 1) * 32, 28, tag);
        STAMP(7);
        const int arow = 32 * wm + r;
        unsigned a_rd[4];
#pragma unroll
        for (int kk = 0; kk < 4; ++kk) a_rd[kk] = wm * 4096 + kk * 1024 + lane * 16;
        struct FragD { Frag4 k0, k1; };
        auto rd = [&](Frag4& f, lds_cptr sb, int k) {
            f.a = cat8(vtr(sb + D_AB + w_rd + (2 * k) * 2048), vtr(sb + D_AB + w_rd + (2 * k) * 2048 + 1024));
            f.b = ldsr128(sb + a_rd[2 * k]);
            f.c = cat8(vtr(sb + D_AB + w_rd + (2 * k + 1) * 2048), vtr(sb + D_AB + w_rd + (2 * k + 1) * 2048 + 1024));
            f.d = ldsr128(sb + a_rd[2 * k + 1]); };
        cons_loop<NT, D_ST, D_SB, 4, 12, 0, FragD>(
            [&](FragD& f, int so, int) { lds_cptr sb = (lds_cptr)lds + so; rd(f.k0, sb, 0); rd(f.k1, sb, 1); },
            [&](const FragD& f) { acc = MFMA32(f.k0.a, f.k0.b, acc); acc = MFMA32(f.k0.c, f.k0.d, acc); acc = MFMA32(f.k1.a, f.k1.b, acc); acc = MFMA32(f.k1.c, f.k1.d, acc); PIN(acc); });
    }
    unsigned woff;
    { const int rw = 8 * w + (lane >> 3); const int ch = (lane & 7) ^ (((rw >> 1) & 1) << 2); woff = rw * 1024 + n0 * 2 + ch * 16; }
    BARRIER();
    STAMP(8);
#pragma unroll
    for (int i = 0; i < 8; ++i) glds16<false>(P.wo + (size_t)i * 64 * 1024, woff, lds0 + i * 8192 + w * 1024);
    float* ex = (float*)(lds + 65536);
    if (wk == 0) {
#pragma unroll
        for (int j = 0; j < 8; ++j) ex[(w * 8 + j) * 64 + lane] = acc[8 + j];
    }
    BARRIER();
    float gate[8];
#pragma unroll
    for (int j = 0; j < 8; ++j) { const float yv = wk ? ex[((w ^ 4) * 8 + j) * 64 + lane] : acc[j]; gate[j] = yv * __builtin_amdgcn_rcpf(1.f + __builtin_amdgcn_exp2f(-1.4426950408889634f * yv)); }
    if constexpr (FUSED) {
        if (blockIdx.x == 0 && w == 7) {
            bool ok = false;
            for (unsigned spin = 0; spin < (1u << 16) && !ok; ++spin) { bool a = true;
#pragma unroll
                for (int i = 0; i < 8; ++i) a &= flag_load(P.ws, FL_START + i * 64 + lane) == tag;
                ok = __all(a); if (!ok) __builtin_amdgcn_s_sleep(8); }
            if (lane == 0) flag_store(P.ws, FL_GEN, tag);
        }
    }
    WAITV(0);
    wait_flags<FUSED>(P.ws, FL_O + (rb >> 2) * 32, 32, tag);
    STAMP(9);
    u32x4 of[16];
    {
        const char* og = P.ws + WS_O + ((size_t)(rb * 2 + wm) * 32 + 16 * wk) * 1024;
#pragma unroll
        for (int i = 0; i < 16; ++i) asm volatile("s_nop 4\n\tglobal_load_dwordx4 %0, %1, %2 offset:%3 sc1" : "=v"(of[i]) : "v"(lane * 16 + (i >> 2) * 4096), "s"(uni(og)), "n"((i & 3) * 1024) : "memory");
    }
    f32x16 accS = {};
#pragma unroll
    for (int i = 0; i < 16; ++i) {
        const bf16x8 wb = cat8(vtr((lds_cptr)lds + w_rd + (16 * wk + i) * 2048), vtr((lds_cptr)lds + w_rd + (16 * wk + i) * 2048 + 1024));
        if (i == 0) asm volatile("s_waitcnt vmcnt(12)" : "+v"(of[0]), "+v"(of[1]), "+v"(of[2]), "+v"(of[3]) :: "memory");
        if (i == 4) asm volatile("s_waitcnt vmcnt(8)" : "+v"(of[4]), "+v"(of[5]), "+v"(of[6]), "+v"(of[7]) :: "memory");
        if (i == 8) asm volatile("s_waitcnt vmcnt(4)" : "+v"(of[8]), "+v"(of[9]), "+v"(of[10]), "+v"(of[11]) :: "memory");
        if (i == 12) asm volatile("s_waitcnt vmcnt(0)" : "+v"(of[12]), "+v"(of[13]), "+v"(of[14]), "+v"(of[15]) :: "memory");
        accS = MFMA32(wb, __builtin_bit_cast(bf16x8, of[i]), accS);
        if ((i & 1) == 1) SBAR();
    }
#pragma unroll
    for (int j = 0; j < 8; ++j) ex[(w * 8 + j) * 64 + lane] = accS[8 * (1 - wk) + j];
    BARRIER();
    char* orow = P.out + (size_t)row * 1024 + (n0 + 32 * wn + 4 * hh + 16 * wk) * 2;
#pragma unroll
    for (int q = 0; q < 2; ++q) {
        float res[4];
#pragma unroll
        for (int j = 0; j < 4; ++j) {
            const float sv = accS[8 * wk + 4 * q + j] + ex[((w ^ 4) * 8 + 4 * q + j) * 64 + lane];
            const float xx = (j & 1) ? bfhi(xv[q][j >> 1]) : bflo(xv[q][j >> 1]);
            res[j] = xx + sv * gate[4 * q + j];
        }
        *(u32x2*)(orow + q * 16) = (u32x2){cvtpk(res[0], res[1]), cvtpk(res[2], res[3])};
    }
    STAMP(10);
}

constexpr float REF_BIAS = 88.f;
__device__ __forceinline__ void attn_slow(const char* kf, const char* vf, const bf16x8 qf, f32x16& o, int lane) {
    const int r = lane & 31;
    float m = -INFINITY; f32x16 oa = {};
    for (int t = 0; t < 64; ++t) {
        u32x4 kq, va0, va1;
        asm volatile("s_nop 4\n\tglobal_load_dwordx4 %0, %3, %4 sc1\n\tglobal_load_dwordx4 %1, %5, %6 sc1\n\tglobal_load_dwordx4 %2, %5, %6 offset:512 sc1\n\ts_waitcnt vmcnt(0)"
                     : "=&v"(kq), "=&v"(va0), "=&v"(va1) : "v"(t * 1024 + lane * 16), "s"(uni(kf)), "v"(t * 1024 + (lane >> 5) * 256 + (r & 15) * 16), "s"(uni(vf)) : "memory");
        f32x16 s = MFMA32(__builtin_bit_cast(bf16x8, kq), qf, (f32x16){});
        float tm = s[0];
#pragma unroll
        for (int i = 1; i < 16; ++i) tm = fmaxf(tm, s[i]);
        tm = swap_max(tm);
        const float mn = fmaxf(m, tm);
        const float al = __builtin_amdgcn_exp2f(m - mn);
        m = mn;
#pragma unroll
        for (int i = 0; i < 16; ++i) oa[i] *= al;
        unsigned pk[8];
#pragma unroll
        for (int i = 0; i < 8; ++i) pk[i] = cvtpk(__builtin_amdgcn_exp2f(s[2 * i] - mn), __builtin_amdgcn_exp2f(s[2 * i + 1] - mn));
        if (r >= 16) { const unsigned c = (r == 16 || r == 20) ? 0x3f803f80u : 0u; va0 = (u32x4){c, c, c, c}; va1 = va0; }
        oa = MFMA32(__builtin_bit_cast(bf16x8, va0), __builtin_bit_cast(bf16x8, ((u32x4){pk[0], pk[1], pk[2], pk[3]})), oa);
        oa = MFMA32(__builtin_bit_cast(bf16x8, va1), __builtin_bit_cast(bf16x8, ((u32x4){pk[4], pk[5], pk[6], pk[7]})), oa);
    }
    o = oa;
}

constexpr int A_SLOT = 16384, A_CONST = 65536;
template <bool FUSED>
__device__ __forceinline__ void phase_attn(const Params& P, char* lds, u64 tag) {
    const int tid = threadIdx.x, lane = tid & 63, r = lane & 31, hh = lane >> 5;
    const int w = __builtin_amdgcn_readfirstlane(tid >> 6);
    const int b = blockIdx.x & 255, xcd = b & 7, bi = b >> 3, head = xcd * 4 + (bi & 3), qb = bi >> 2;
    const unsigned lds0 = (unsigned)(uintptr_t)lds;
    const char* kf = P.ws + WS_K + (size_t)head * 65536; const char* vf = P.ws + WS_V + (size_t)head * 65536;
    { const int row = tid & 15; const unsigned c = (row == 0 || row == 4) ? 0x3f803f80u : 0u; *(u32x4*)(lds + A_CONST + tid * 16) = (u32x4){c, c, c, c}; }
    if constexpr (FUSED) { PIN(tag); WAITV(0); }
    wait_flags<FUSED>(P.ws, FL_Q + (head >> 2) * 32, 32, tag);
    if constexpr (FUSED) { if (threadIdx.x == 0) flag_store(P.ws, FL_START + blockIdx.x, tag); }
    STAMP(1);
    auto issue = [&](int ci) {
        const int c = (qb + ci) & 7; const unsigned sb = lds0 + (ci & 3) * A_SLOT;
        glds16<true>(kf + c * 8192 + w * 1024, lane * 16, sb + w * 1024);
        glds16<true>(vf + c * 8192 + w * 1024, lane * 16, sb + 8192 + w * 1024);
    };
    issue(0);
    u32x4 qv;
    asm volatile("s_nop 4\n\tglobal_load_dwordx4 %0, %1, %2 sc1" : "=v"(qv) : "v"(lane * 16), "s"(uni(P.ws + WS_Q + (size_t)(head * 64 + qb * 8 + w) * 1024)) : "memory");
    issue(1); issue(2); issue(3);
    asm volatile("s_waitcnt vmcnt(6)" : "+v"(qv) :: "memory");
    BARRIER();
    STAMP(2);
    const bf16x8 qf = __builtin_bit_cast(bf16x8, qv);
    const unsigned kaddr = lane * 16;
    const unsigned vaddr = (r < 16) ? (8192 + hh * 256 + r * 16) : (A_CONST + hh * 256 + (r - 16) * 16);
    const unsigned vmul = (r < 16) ? 1u : 0u;
    f32x16 o, cneg, sA, sB;
    {
        const bf16x8 k0 = ldsr128((lds_cptr)lds + kaddr), k1 = ldsr128((lds_cptr)lds + kaddr + 1024);
        const f32x16 s0 = MFMA32(k0, qf, (f32x16){}), s1 = MFMA32(k1, qf, (f32x16){});
        float tm = fmaxf(s0[0], s1[0]);
#pragma unroll
        for (int i = 1; i < 16; ++i) tm = fmaxf(tm, fmaxf(s0[i], s1[i]));
        tm = swap_max(tm);
        const float c = -(tm + REF_BIAS);
#pragma unroll
        for (int i = 0; i < 16; ++i) { cneg[i] = c; o[i] = 0.f; }
        PIN(cneg);
        sA = MFMA32(k0, qf, cneg);
    }
    lds_cptr kp = (lds_cptr)lds + kaddr;
    lds_cptr vp = (lds_cptr)lds + vaddr;
    bf16x8 kfr = ldsr128(kp + 1024);
    bf16x8 v0f = ldsr128(vp), v1f;
    { u32x4 z = {0u, 0u, 0u, 0u}; v1f = __builtin_bit_cast(bf16x8, z); }
    u32x4 pk0 = {0u, 0u, 0u, 0u}, pk1 = {0u, 0u, 0u, 0u};
#define EXP2(x) __builtin_amdgcn_exp2f(x)
#define EX(dst, src) do { dst = EXP2(src); PIN(dst); } while (0)
#define CV(dst, a, b) do { dst = cvtpk(a, b); PIN(dst); } while (0)
#define BF(x) __builtin_bit_cast(bf16x8, x)
#define STEP(cur, nxt, KN, V1, V0N) do { \
        float e[16]; \
        nxt = MFMA32(kfr, qf, cneg); PIN(nxt); SBAR(); \
        PIN(cur); kfr = ldsr128(KN); SBAR(); \
        EX(e[0], cur[0]); EX(e[1], cur[1]); EX(e[2], cur[2]); EX(e[3], cur[3]); EX(e[4], cur[4]); EX(e[5], cur[5]); EX(e[6], cur[6]); SBAR(); \
        o = MFMA32(v1f, BF(pk1), o); PIN(o); SBAR(); \
        PIN(cur); PIN(e[0]); PIN(e[2]); PIN(e[4]); PIN(e[6]); v1f = ldsr128(V1); SBAR(); \
        EX(e[7], cur[7]); CV(pk0[0], e[0], e[1]); CV(pk0[1], e[2], e[3]); CV(pk0[2], e[4], e[5]); \
        EX(e[8], cur[8]); CV(pk0[3], e[6], e[7]); EX(e[9], cur[9]); EX(e[10], cur[10]); EX(e[11], cur[11]); SBAR(); \
        o = MFMA32(v0f, BF(pk0), o); PIN(o); SBAR(); \
        PIN(cur); PIN(e[8]); PIN(e[10]); v0f = ldsr128(V0N); SBAR(); \
        EX(e[12], cur[12]); EX(e[13], cur[13]); EX(e[14], cur[14]); EX(e[15], cur[15]); \
        CV(pk1[0], e[8], e[9]); CV(pk1[1], e[10], e[11]); CV(pk1[2], e[12], e[13]); CV(pk1[3], e[14], e[15]); SBAR(); \
    } while (0)
    __builtin_amdgcn_s_setprio(APRIO);
    for (int ci = 0; ci < 8; ++ci) {
        lds_cptr kc = kp + (ci & 3) * A_SLOT, kn = kp + ((ci + 1) & 3) * A_SLOT;
        lds_cptr vc = vp + vmul * ((ci & 3) * A_SLOT), vn = vp + vmul * (((ci + 1) & 3) * A_SLOT);
        STEP(sA, sB, kc + 2 * 1024, vc + 0 * 1024 + 512, vc + 1 * 1024);
        STEP(sB, sA, kc + 3 * 1024, vc + 1 * 1024 + 512, vc + 2 * 1024);
        STEP(sA, sB, kc + 4 * 1024, vc + 2 * 1024 + 512, vc + 3 * 1024);
        STEP(sB, sA, kc + 5 * 1024, vc + 3 * 1024 + 512, vc + 4 * 1024);
        STEP(sA, sB, kc + 6 * 1024, vc + 4 * 1024 + 512, vc + 5 * 1024);
        STEP(sB, sA, kc + 7 * 1024, vc + 5 * 1024 + 512, vc + 6 * 1024);
        if (ci < 5) WAITV(2); else WAITV(0);
        BARRIER();
        if (ci >= 1 && ci + 3 < 8) issue(ci + 3);
        STEP(sA, sB, kn + 0 * 1024, vc + 6 * 1024 + 512, vc + 7 * 1024);
        STEP(sB, sA, kn + 1 * 1024, vc + 7 * 1024 + 512, vn);
    }
    o = MFMA32(v1f, BF(pk1), o);
    STAMP(3);
    bool bad = !(o[8] < 1.0e37f) || !(o[8] > 0.f);
#pragma unroll
    for (int i = 0; i < 8; ++i) bad |= !(fabsf(o[i]) < 3.0e38f);
    if (__any(bad)) attn_slow(kf, vf, qf, o, lane);
    const float rl = 1.0f / o[8];
    const u32x4 pk = {cvtpk(o[0] * rl, o[1] * rl), cvtpk(o[2] * rl, o[3] * rl), cvtpk(o[4] * rl, o[5] * rl), cvtpk(o[6] * rl, o[7] * rl)};
    st16_sc1(P.ws + WS_O, (unsigned)(((qb * 8 + w) * 32 + head) * 64 + lane) * 16, pk);
    if constexpr (FUSED) publish(P.ws, FL_O + qb * 32 + head, tag);
    STAMP(4);
}

template <bool FUSED>
__device__ __forceinline__ void phase_side(const Params& P, char* lds, u64 tag) {
    const int tid = threadIdx.x, lane = tid & 63;
    const int w = __builtin_amdgcn_readfirstlane(tid >> 6);
    const int j = blockIdx.x & 255;
    const int row = 8 * j + w;
    const unsigned tg = (unsigned)tag;
    const u32x4 xr = *(const u32x4*)(P.x + (size_t)row * 1024 + lane * 16);
    const bool has = j < 96;
    const int cgp = j % 24, sl = j / 24;
    u32x4 wv[2] = {};
    if (has) {
#pragma unroll
        for (int i = 0; i < 2; ++i) wv[i] = *(const u32x4*)(P.wqkv + (size_t)(128 * sl + 16 * w + 8 * i + (lane >> 3)) * 3072 + cgp * 128 + (lane & 7) * 16);
    }
    float v[8];
#pragma unroll
    for (int i = 0; i < 4; ++i) { v[2 * i] = bflo(xr[i]); v[2 * i + 1] = bfhi(xr[i]); }
    float sx = ((v[0] + v[1]) + (v[2] + v[3])) + ((v[4] + v[5]) + (v[6] + v[7]));
    sx = wave_sum(sx);
    STAMP(5);
    const float mu = sx * (1.f / 512.f);
    float sq = 0.f;
#pragma unroll
    for (int i = 0; i < 8; ++i) { v[i] -= mu; sq = fmaf(v[i], v[i], sq); }
    sq = wave_sum(sq);
    const float rstd = __builtin_amdgcn_rsqf(sq * (1.f / 512.f) + 1e-5f);
    if (lane == 0) st16_sc1(P.ws + WS_GST, (unsigned)row * 16, (u32x4){__float_as_uint(mu), tg, __float_as_uint(rstd), tg});
    const u32x4 pk = {cvtpk(v[0] * rstd, v[1] * rstd), cvtpk(v[2] * rstd, v[3] * rstd), cvtpk(v[4] * rstd, v[5] * rstd), cvtpk(v[6] * rstd, v[7] * rstd)};
    st16_sc1(P.ws + WS_XN, (unsigned)((((lane >> 2) * 2048 + row) * 4 + ((lane & 3) ^ ((row >> 2) & 3))) * 16), pk);
    if constexpr (FUSED) publish(P.ws, FL_S + j, tag); else { WAITV(0); BARRIER(); }
    STAMP(7);
    if (has) {
        float a[8];
#pragma unroll
        for (int k = 0; k < 4; ++k) { a[2 * k] = bflo(wv[0][k]) + bflo(wv[1][k]); a[2 * k + 1] = bfhi(wv[0][k]) + bfhi(wv[1][k]); }
#pragma unroll
        for (int k = 0; k < 8; ++k) a[k] = swap_add(swap16_add(dpp_add<0x128>(a[k])));
        float* red = (float*)lds;
        if (lane < 8) { *(f32x4*)&red[w * 64 + lane * 8] = (f32x4){a[0], a[1], a[2], a[3]}; *(f32x4*)&red[w * 64 + lane * 8 + 4] = (f32x4){a[4], a[5], a[6], a[7]}; }
        BARRIER();
        if (tid < 32) {
            f32x2 c = {0.f, 0.f};
#pragma unroll
            for (int i = 0; i < 8; ++i) c += *(const f32x2*)&red[i * 64 + 2 * tid];
            st16_sc1(P.ws + WS_GCS, (unsigned)(sl * 1536 + cgp * 64 + 2 * tid) * 8, (u32x4){__float_as_uint(c[0]), tg, __float_as_uint(c[1]), tg});
        }
    }
    STAMP(6);
    BARRIER();
}

template <int MASK, bool FUSED>
__global__ __launch_bounds__(512, 4) void mega(Params P) {
    extern __shared__ __attribute__((aligned(16))) char lds[];
    u64 tag = 0;
    if constexpr (FUSED) {
        tag = flag_load(P.ws, FL_GEN) + 1;
    }
    STAMP((MASK & 1) ? 0 : (MASK & 2) ? 11 : (MASK & 8) ? 14 : 12);
    if (blockIdx.x < 256) {
        if constexpr (MASK & 1) phase_q<FUSED>(P, lds, tag);
        if constexpr (MASK & 2) phase_u<FUSED>(P, lds, tag);
        if constexpr (MASK & 4) { BARRIER(); phase_d<FUSED>(P, lds, tag); }
    } else {
        if constexpr (MASK & 1) phase_side<FUSED>(P, lds, tag);
        if constexpr (MASK & 2) phase_u<FUSED>(P, lds, tag);
        if constexpr (MASK & 8) phase_attn<FUSED>(P, lds, tag);
    }
}

extern "C" void kernel_launch(void* const* d_in, const int* in_sizes, int n_in, void* d_out, int out_size, void* d_ws, size_t ws_size, hipStream_t stream) {
    Params P;
    P.x = (const char*)d_in[0]; P.wqkv = (const char*)d_in[1]; P.wo = (const char*)d_in[2];
    P.wg = (const char*)d_in[4]; P.wu = (const char*)d_in[5]; P.wd = (const char*)d_in[6];
    P.out = (char*)d_out; P.ws = (char*)d_ws;
    (void)hipFuncSetAttribute((const void*)mega<15, true>, hipFuncAttributeMaxDynamicSharedMemorySize, LDS_BYTES);
    mega<15, true><<<512, 512, LDS_BYTES, stream>>>(P);
}
```

```cpp
#define NO_PF
#define WPF 1
#define APRIO 1
#include <hip/hip_runtime.h>
#include <hip/hip_bf16.h>
#include <stdint.h>

typedef short bf16x8 __attribute__((ext_vector_type(8)));
typedef short s16x4 __attribute__((ext_vector_type(4)));
typedef short v4i16_t __attribute__((ext_vector_type(4)));
typedef float f32x16 __attribute__((ext_vector_type(16)));
typedef float f32x4 __attribute__((ext_vector_type(4)));
typedef float f32x2 __attribute__((ext_vector_type(2)));
typedef unsigned u32x4 __attribute__((ext_vector_type(4)));
typedef unsigned u32x2 __attribute__((ext_vector_type(2)));
typedef __bf16 bf2_t __attribute__((ext_vector_type(2)));
typedef unsigned long long u64;
#define LDSP __attribute__((address_space(3)))
#define GLBP __attribute__((address_space(1)))
typedef LDSP const char* lds_cptr;
typedef LDSP char* lds_ptr;

constexpr int SEQ = 2048, DM = 512, DF = 1792, NH = 32;
constexpr size_t WS_Q = 0, WS_K = 2u << 20, WS_V = 4u << 20, WS_O = 6u << 20, WS_H = 8u << 20, WS_STATS = 16u << 20, WS_FLAGS = (16u << 20) + 131072;
constexpr int FL_GEN = 0, FL_START = 16, FL_Q = 16 + 512, FL_H = FL_Q + 256, FL_O = FL_H + 512, FL_S = FL_O + 256, FL_QL = FL_S + 256, FL_END = FL_QL + 256;
constexpr size_t WS_GST = (16u << 20), WS_GCS = (16u << 20) + 32768;
constexpr size_t WS_XN = 18u << 20;
constexpr float QSCALE = 0.25f * 1.4426950408889634f;
constexpr int LDS_BYTES = 81920;

#define MFMA32(a, b, c) __builtin_amdgcn_mfma_f32_32x32x16_bf16((a), (b), (c), 0, 0, 0)
#define SBAR() __builtin_amdgcn_sched_barrier(0)
#define PIN(x) asm volatile("" : "+v"(x))

__device__ __forceinline__ unsigned cvtpk(float lo, float hi) { return __builtin_bit_cast(unsigned, __builtin_convertvector(((f32x2){lo, hi}), bf2_t)); }
__device__ __forceinline__ unsigned uni(unsigned v) { return (unsigned)__builtin_amdgcn_readfirstlane((int)v); }
template <class T> __device__ __forceinline__ T* uni(T* p) { const u64 v = (u64)p; return (T*)(((u64)uni((unsigned)(v >> 32)) << 32) | uni((unsigned)v)); }
#define GLDS_ASM(SC) \
    if constexpr (NP == 1) asm volatile("s_mov_b32 m0, %2\n\ts_nop 0\n\tglobal_load_lds_dwordx4 %0, %1" SC :: "v"(v0), "s"(gbase), "s"(lds_base) : "memory"); \
    else if constexpr (NP == 2) asm volatile("s_mov_b32 m0, %3\n\ts_nop 0\n\tglobal_load_lds_dwordx4 %0, %2" SC "\n\tglobal_load_lds_dwordx4 %1, %2 offset:1024" SC :: "v"(v0), "v"(v1), "s"(gbase), "s"(lds_base) : "memory"); \
    else if constexpr (NP == 3) asm volatile("s_mov_b32 m0, %4\n\ts_nop 0\n\tglobal_load_lds_dwordx4 %0, %3" SC "\n\tglobal_load_lds_dwordx4 %1, %3 offset:1024" SC "\n\tglobal_load_lds_dwordx4 %2, %3 offset:2048" SC :: "v"(v0), "v"(v1), "v"(v2), "s"(gbase), "s"(lds_base) : "memory"); \
    else asm volatile("s_mov_b32 m0, %5\n\ts_nop 0\n\tglobal_load_lds_dwordx4 %0, %4" SC "\n\tglobal_load_lds_dwordx4 %1, %4 offset:1024" SC "\n\tglobal_load_lds_dwordx4 %2, %4 offset:2048" SC "\n\tglobal_load_lds_dwordx4 %3, %4 offset:3072" SC :: "v"(v0), "v"(v1), "v"(v2), "v"(v3), "s"(gbase), "s"(lds_base) : "memory");
template <bool SC1, int NP>
__device__ __forceinline__ void gldsN(const void* gbase, unsigned lds_base, unsigned v0, unsigned v1 = 0, unsigned v2 = 0, unsigned v3 = 0) {
    if constexpr (SC1) { GLDS_ASM(" sc1") } else { GLDS_ASM("") }
}
template <bool SC1>
__device__ __forceinline__ void glds16(const void* gbase, unsigned voff, unsigned lds_base) { gldsN<SC1, 1>(gbase, lds_base, voff); }
__device__ __forceinline__ void st16_sc1(void* gbase, unsigned voff, u32x4 v) { asm volatile("s_nop 4\n\tglobal_store_dwordx4 %0, %1, %2 sc1\n\ts_nop 1" :: "v"(voff), "v"(v), "s"(uni(gbase)) : "memory"); }
__device__ __forceinline__ void st8_sc1(void* gbase, unsigned voff, f32x2 v) { asm volatile("s_nop 4\n\tglobal_store_dwordx2 %0, %1, %2 sc1\n\ts_nop 1" :: "v"(voff), "v"(v), "s"(uni(gbase)) : "memory"); }
__device__ __forceinline__ bf16x8 ldsr128(lds_cptr p) { return *(const LDSP bf16x8*)p; }
__device__ __forceinline__ s16x4 vtr(lds_cptr p) { return __builtin_bit_cast(s16x4, __builtin_amdgcn_ds_read_tr16_b64_v4i16((LDSP v4i16_t*)p)); }
__device__ __forceinline__ bf16x8 cat8(s16x4 a, s16x4 b) { return (bf16x8){a[0], a[1], a[2], a[3], b[0], b[1], b[2], b[3]}; }
__device__ __forceinline__ float dot2(unsigned a, unsigned b, float c) { return __builtin_amdgcn_fdot2_f32_bf16(__builtin_bit_cast(bf2_t, a), __builtin_bit_cast(bf2_t, b), c, false); }
__device__ __forceinline__ float swap_add(float v) {
    auto rr = __builtin_amdgcn_permlane32_swap(__float_as_uint(v), __float_as_uint(v), false, false);
    return __uint_as_float(rr[0]) + __uint_as_float(rr[1]);
}
__device__ __forceinline__ float swap_max(float v) {
    auto rr = __builtin_amdgcn_permlane32_swap(__float_as_uint(v), __float_as_uint(v), false, false);
    return __builtin_fmaxf(__uint_as_float(rr[0]), __uint_as_float(rr[1]));
}
template <int CTRL> __device__ __forceinline__ float dpp_add(float v) { return v + __int_as_float(__builtin_amdgcn_update_dpp(0, __float_as_int(v), CTRL, 0xf, 0xf, true)); }
__device__ __forceinline__ float swap16_add(float v) {
    auto rr = __builtin_amdgcn_permlane16_swap(__float_as_uint(v), __float_as_uint(v), false, false);
    return __uint_as_float(rr[0]) + __uint_as_float(rr[1]);
}
__device__ __forceinline__ float wave_sum(float v) {
    v = dpp_add<0xB1>(v); v = dpp_add<0x4E>(v); v = dpp_add<0x141>(v); v = dpp_add<0x140>(v);
    return swap_add(swap16_add(v));
}
#define WAITV(n) asm volatile("s_waitcnt vmcnt(" #n ")" ::: "memory")
#define BARRIER() asm volatile("s_waitcnt lgkmcnt(0)\n\ts_barrier" ::: "memory")
__device__ __forceinline__ float bflo(unsigned u) { return __uint_as_float(u << 16); }
__device__ __forceinline__ float bfhi(unsigned u) { return __uint_as_float(u & 0xffff0000u); }

#define STAMP(slot) do {} while (0)

struct Params { const char* x; const char* wqkv; const char* wo; const char* wg; const char* wu; const char* wd; char* out; char* ws; };

__device__ __forceinline__ u64 flag_load(const char* ws, int idx) { return __hip_atomic_load((const GLBP u64*)(ws + WS_FLAGS) + idx, __ATOMIC_RELAXED, __HIP_MEMORY_SCOPE_AGENT); }
__device__ __forceinline__ void flag_store(char* ws, int idx, u64 v) { __hip_atomic_store((GLBP u64*)(ws + WS_FLAGS) + idx, v, __ATOMIC_RELAXED, __HIP_MEMORY_SCOPE_AGENT); }
__device__ __forceinline__ void publish(char* ws, int idx, u64 tag) {
    WAITV(0); BARRIER();
    if (threadIdx.x == 0) flag_store(ws, idx, tag);
}
template <bool WAIT>
__device__ __forceinline__ void wait_flags(char* ws, int idx, int n, u64 tag) {
    if constexpr (WAIT) {
        if (threadIdx.x < 64) {
            const int i = idx + ((int)threadIdx.x < n ? (int)threadIdx.x : 0);
            for (unsigned spin = 0; spin < (1u << 22); ++spin) {
                const u64 v = flag_load(ws, i);
                if (__all(v == tag)) break;
                __builtin_amdgcn_s_sleep(2);
            }
        }
    }
    BARRIER();
}

__device__ __forceinline__ void waitv_n(int n) {
    if (n <= 0) WAITV(0); else if (n == 1) WAITV(1); else if (n == 2) WAITV(2); else if (n == 3) WAITV(3); else if (n == 4) WAITV(4); else if (n == 5) WAITV(5);
    else if (n == 6) WAITV(6); else if (n == 7) WAITV(7); else if (n == 8) WAITV(8); else if (n == 9) WAITV(9); else WAITV(10);
}
#define SGB(mask, n) __builtin_amdgcn_sched_group_barrier(mask, n, 0)
template <int NM, int NR, int NV>
__device__ __forceinline__ void interleave() {
    if constexpr (NV < 0) {
#pragma unroll
        for (int i = 0; i < NM; ++i) { SGB(0x8, 1); if (3 * i < NR) SGB(0x100, 3); }
    } else {
#pragma unroll
        for (int i = 0; i < NM; ++i) { SGB(0x8, 1); SGB(0x100, (NR + NM - 1) / NM); if (NV) SGB(0x2, (NV + NM - 1) / NM); }
    }
}
template <int N, int D, int NPW, class Issue> __device__ __forceinline__ void loader_loop(Issue issue, u64* dbg = nullptr);
#define TS(t) asm volatile("s_memtime %0\n\ts_waitcnt lgkmcnt(0)" : "=s"(t) :: "memory")
template <int D, int NPW, int SB, int NM, int NR, int NV, class Frag, class Issue, class Read, class Mma, class After>
__device__ __forceinline__ void gemm_loop_rt(const int N, Issue issue, Read read, Mma mma, After after) {
    Frag f0, f1;
    if constexpr (NPW > 0) waitv_n((D - 1) * NPW);
    BARRIER();
    read(f0, 0, 0, 0);
    int rs = 0;
#pragma nounroll
    for (int s = 0; s < N - D; ++s) {
        const int ns = rs == D - 1 ? 0 : rs + 1;
        read(f1, rs * SB, 1, s); mma(f0, 0); interleave<NM, NR, NV>(); SBAR();
        if constexpr (NPW > 0) waitv_n((D - 2) * NPW);
        BARRIER();
        issue(s + D, rs); SBAR();
        read(f0, ns * SB, 0, s + 1); mma(f1, 1); interleave<NM, NR, NV>(); SBAR();
        after(s);
        rs = ns;
    }
#pragma unroll
    for (int t = 0; t < D; ++t) {
        const int ns = rs == D - 1 ? 0 : rs + 1;
        read(f1, rs * SB, 1, N - D + t); mma(f0, 0); interleave<NM, NR, NV>(); SBAR();
        if (t < D - 1) { if constexpr (NPW > 0) waitv_n((D - 2 - t) * NPW); BARRIER(); read(f0, ns * SB, 0, N - D + t + 1); }
        mma(f1, 1); if (t < D - 1) interleave<NM, NR, NV>(); SBAR();
        rs = ns;
    }
}
template <int D, int NPW, class Issue>
__device__ __forceinline__ void loader_loop_rt(const int N, Issue issue) {
    waitv_n((D - 1) * NPW); BARRIER();
    int rs = 0;
#pragma nounroll
    for (int s = 0; s < N - D; ++s) {
        waitv_n((D - 2) * NPW); BARRIER();
        issue(s + D, rs);
        rs = rs == D - 1 ? 0 : rs + 1;
    }
#pragma unroll
    for (int t = 0; t < D - 1; ++t) { waitv_n((D - 2 - t) * NPW); BARRIER(); }
}
template <int N, int D, int NPW, int SB, int NM, int NR, int NV, class Frag, class Issue, class Read, class Mma>
__device__ __forceinline__ void gemm_loop(Issue issue, Read read, Mma mma, u64* dbg = nullptr) {
    Frag f0, f1;
    waitv_n((D - 1) * NPW); BARRIER();
    read(f0, 0, 0, 0);
    int rs = 0;
#pragma nounroll
    for (int s = 0; s < N - D; ++s) {
        const int ns = rs == D - 1 ? 0 : rs + 1;
        read(f1, rs * SB, 1, s); mma(f0, 0); interleave<NM, NR, NV>(); SBAR();
        waitv_n((D - 2) * NPW); BARRIER();
        issue(s + D, rs); SBAR();
        read(f0, ns * SB, 0, s + 1); mma(f1, 1); interleave<NM, NR, NV>(); SBAR();
        rs = ns;
    }
#pragma unroll
    for (int t = 0; t < D; ++t) {
        const int ns = rs == D - 1 ? 0 : rs + 1;
        read(f1, rs * SB, 1, N - D + t); mma(f0, 0); interleave<NM, NR, NV>(); SBAR();
        if (t < D - 1) { waitv_n((D - 2 - t) * NPW); BARRIER(); read(f0, ns * SB, 0, N - D + t + 1); }
        mma(f1, 1); if (t < D - 1) interleave<NM, NR, NV>(); SBAR();
        rs = ns;
    }
}
template <int N, int D, int SB, int NM, int NR, int NV, class Frag, class Read, class Mma>
__device__ __forceinline__ void cons_loop(Read read, Mma mma, char* wsp = nullptr, int slot0 = 0) {
    static_assert(N % 2 == 0, "");
    Frag fa, fb;
    BARRIER();
    read(fa, 0, 0);
    int ns = 1;
#pragma nounroll
    for (int s = 0; s < N - 2; s += 2) {
        BARRIER();
        read(fb, ns * SB, s + 1); mma(fa); interleave<NM, NR, NV>(); SBAR();
        ns = ns == D - 1 ? 0 : ns + 1;
        BARRIER();
        read(fa, ns * SB, s + 2); mma(fb); interleave<NM, NR, NV>(); SBAR();
        ns = ns == D - 1 ? 0 : ns + 1;
    }
    BARRIER();
    read(fb, ns * SB, N - 1); mma(fa); interleave<NM, NR, NV>(); SBAR();
    mma(fb); SBAR();
}
template <int N, int D, int NPW, class Issue>
__device__ __forceinline__ void loader_loop(Issue issue, u64* dbg) {
    waitv_n((D - 1) * NPW); BARRIER();
    int rs = 0;
#pragma nounroll
    for (int s = 0; s < N - D; ++s) {
        waitv_n((D - 2) * NPW);
        BARRIER();
        issue(s + D, rs);
        rs = rs == D - 1 ? 0 : rs + 1;
    }
#pragma unroll
    for (int t = 0; t < D - 1; ++t) { waitv_n((D - 2 - t) * NPW); BARRIER(); }
}
struct Frag2 { bf16x8 a, b; };
struct Frag3 { bf16x8 a, b, c; };
struct Frag4 { bf16x8 a, b, c, d; };
__device__ __forceinline__ float sum2x4(bf16x8 v, float acc) { const u32x4 u = __builtin_bit_cast(u32x4, v);
    acc = dot2(u[0], 0x3f803f80u, acc); acc = dot2(u[1], 0x3f803f80u, acc); acc = dot2(u[2], 0x3f803f80u, acc); return dot2(u[3], 0x3f803f80u, acc); }
__device__ __forceinline__ float sq2x4(bf16x8 v, float acc) { const u32x4 u = __builtin_bit_cast(u32x4, v);
    acc = dot2(u[0], u[0], acc); acc = dot2(u[1], u[1], acc); acc = dot2(u[2], u[2], acc); return dot2(u[3], u[3], acc); }

constexpr int Q_ST = 4, Q_WB = 12288, Q_A0 = Q_ST * Q_WB, Q_ACH = 8192, Q_SC = Q_A0 + 3 * Q_ACH;
template <bool FUSED>
__device__ __forceinline__ void phase_q(const Params& P, char* lds, u64 tag) {
    const int tid = threadIdx.x, lane = tid & 63, r = lane & 31, hh = lane >> 5;
    const int w = __builtin_amdgcn_readfirstlane(tid >> 6);
    const int b = blockIdx.x & 255, xcd = b & 7, bi = b >> 3;
    const int ri = bi & 7, cj = bi >> 3;
    const int rb = 8 * (2 * (xcd & 1) + ((xcd >> 1) & 1)) + ri, cg = 4 * (xcd >> 2) + cj;
    const int kc0 = 0;
    const int m0 = rb * 64;
    const unsigned lds0 = (unsigned)(uintptr_t)lds;
    float* sc = (float*)(lds + Q_SC);
    if (w >= 4) {
        const int p = w - 4;
        unsigned woff[3];
        unsigned aoff0;
        { const int row = 16 * p + (lane >> 3), c = (lane & 7) ^ ((row >> 1) & 7); aoff0 = row * 1024 + c * 16; }
#pragma unroll
        for (int i = 0; i < 3; ++i) { const int pc = 64 * (3 * p + i) + lane, row = pc / 24, ch = pc % 24, cs = (ch & 7) ^ (((row >> 1) & 1) << 2); woff[i] = row * 3072 + (ch >> 3) * 1024 + cg * 128 + cs * 16; }
        woff[1] -= 1024; woff[2] -= 2048;
        const char* ag = uni(P.x + (size_t)m0 * 1024);
        const char* wq = uni(P.wqkv);
        const unsigned ldsW = uni(lds0 + 3 * p * 1024), ldsX = uni(lds0 + Q_A0 + 2 * p * 1024);
        auto issueW = [&](int st, int slot) { const int kt = (2 * kc0 + st) & 15; gldsN<false, 3>(wq + kt * (32 * 3072), ldsW + slot * Q_WB, woff[0], woff[1], woff[2]); };
        auto issueX = [&](int c, int cslot, int set) { const int kc = (kc0 + c) & 7; glds16<false>(ag + kc * 128 + set * 8192, aoff0 ^ (unsigned)(set << 6), ldsX + cslot * Q_ACH + set * 1024); };
        issueX(0, 0, 0); issueX(0, 0, 1); issueW(0, 0); issueX(1, 1, 0); issueX(1, 1, 1); issueW(1, 1); issueW(2, 2); issueX(2, 2, 0); issueX(2, 2, 1); issueW(3, 3);
        loader_loop<16, Q_ST, 4>([&](int st, int slot) {
            const int u = st + 1; const int c = (u >> 1) < 7 ? (u >> 1) : 7;
            issueW(st, slot); issueX(c, c % 3, u & 1); }, (u64*)(P.ws + (40u << 20)) + (size_t)(blockIdx.x * 8 + w) * 64);
        if constexpr (FUSED) { if (w == 4 && lane == 0) flag_store(P.ws, FL_QL + b, tag); }
        {
            const unsigned tg = (unsigned)tag;
            if (p == 0) {
                u32x4 v;
                for (unsigned spin = 0; spin < (1u << 16); ++spin) {
                    asm volatile("s_nop 4\n\tglobal_load_dwordx4 %0, %1, %2 sc1\n\ts_waitcnt vmcnt(0)" : "=v"(v) : "v"((unsigned)(m0 + lane) * 16), "s"(uni(P.ws + WS_GST)) : "memory");
                    if (__all(v[1] == tg && v[3] == tg)) break;
                    __builtin_amdgcn_s_sleep(1);
                }
                *(f32x2*)&sc[lane * 2] = (f32x2){__uint_as_float(v[0]), __uint_as_float(v[2])};
            } else {
                u32x4 v0, v1;
                const unsigned off = (unsigned)(((lane >> 5) * 2) * 1536 + (p - 1) * 512 + cg * 64 + 2 * (lane & 31)) * 8;
                for (unsigned spin = 0; spin < (1u << 16); ++spin) {
                    asm volatile("s_nop 4\n\tglobal_load_dwordx4 %0, %2, %3 sc1\n\tglobal_load_dwordx4 %1, %4, %3 sc1\n\ts_waitcnt vmcnt(0)" : "=&v"(v0), "=&v"(v1) : "v"(off), "s"(uni(P.ws + WS_GCS)), "v"(off + 1536 * 8) : "memory");
                    if (__all(v0[1] == tg && v0[3] == tg && v1[1] == tg && v1[3] == tg)) break;
                    __builtin_amdgcn_s_sleep(1);
                }
                const float c0 = swap_add(__uint_as_float(v0[0]) + __uint_as_float(v1[0])), c1 = swap_add(__uint_as_float(v0[2]) + __uint_as_float(v1[2]));
                if (lane < 32) *(f32x2*)&sc[128 + (p - 1) * 64 + 2 * lane] = (f32x2){c0, c1};
            }
        }
        BARRIER();
        STAMP(2);
    } else {
        const int rt = w >> 1, hf = w & 1;
        const int arow = 32 * rt + r;
        unsigned a_rd[2];
#pragma unroll
        for (int k = 0; k < 2; ++k) a_rd[k] = Q_A0 + arow * 128 + ((2 * k + hh) ^ ((arow >> 1) & 7)) * 16;
        const int q4 = (lane & 15) >> 2;
        const unsigned chw = (4 * hf + 2 * ((lane >> 4) & 1) + ((lane & 3) >> 1)) ^ (((q4 >> 1) & 1) << 2);
        const unsigned w_rd = (8 * hh + q4) * 384 + chw * 16 + 8 * (lane & 1);
        unsigned pf0;
        {
            const int idx = w * 64 + lane;
            const char* a;
            if (idx < 144) { const int st = 4 + idx / 12, l = ri * 12 + idx % 12; a = P.wqkv + (size_t)(st * 32 + l / 3) * 3072 + (l % 3) * 1024 + cg * 128; }
            else { const int i2 = idx < 224 ? idx - 144 : 0; a = P.x + (size_t)(m0 + cj * 16 + (i2 & 15)) * 1024 + (3 + (i2 >> 4)) * 128; }
            pf0 = 0;
        }
        f32x16 accQ = {}, accK = {}, accV = {};
        struct FragQ { Frag4 k0, k1; };
        auto rd = [&](Frag4& f, lds_cptr xa, lds_cptr wb) { f.a = ldsr128(xa);
            f.b = cat8(vtr(wb), vtr(wb + 1536)); f.c = cat8(vtr(wb + 128), vtr(wb + 128 + 1536)); f.d = cat8(vtr(wb + 256), vtr(wb + 256 + 1536)); };
        cons_loop<16, Q_ST, Q_WB, 6, 14, 0, FragQ>(
            [&](FragQ& f, int so, int stg) { lds_cptr sb = (lds_cptr)lds + so + w_rd; lds_cptr xb = (lds_cptr)lds + ((stg >> 1) % 3) * Q_ACH; const unsigned fl = (stg & 1) << 6;
                rd(f.k0, xb + (a_rd[0] ^ fl), sb); rd(f.k1, xb + (a_rd[1] ^ fl), sb + 6144); },
            [&](const FragQ& f) {
                accQ = MFMA32(f.k0.b, f.k0.a, accQ); accK = MFMA32(f.k0.c, f.k0.a, accK); accV = MFMA32(f.k0.a, f.k0.d, accV);
                accQ = MFMA32(f.k1.b, f.k1.a, accQ); accK = MFMA32(f.k1.c, f.k1.a, accK); accV = MFMA32(f.k1.a, f.k1.d, accV);
                PIN(accQ); PIN(accK); PIN(accV); }, P.ws, 1);
        STAMP(13);
        asm volatile("s_waitcnt vmcnt(0)" : "+v"(pf0) :: "memory");
        BARRIER(); SBAR();
        STAMP(2);
        const int tile = rb * 2 + rt, hd0 = cg * 4 + hf * 2;
        {
            const f32x2 st = *(const f32x2*)&sc[arow * 2];
            const float mu = st[0], rstd = st[1];
#pragma unroll
            for (int isk = 0; isk < 2; ++isk) {
                const f32x16& acc = isk ? accK : accQ;
                const float scl = isk ? rstd : rstd * QSCALE;
                unsigned pk[8];
#pragma unroll
                for (int q = 0; q < 4; ++q) {
                    const f32x4 c4 = *(const f32x4*)&sc[128 + isk * 64 + hf * 32 + 8 * q + 4 * hh];
                    const float v0 = (acc[4 * q] - mu * c4[0]) * scl, v1 = (acc[4 * q + 1] - mu * c4[1]) * scl, v2 = (acc[4 * q + 2] - mu * c4[2]) * scl, v3 = (acc[4 * q + 3] - mu * c4[3]) * scl;
                    pk[2 * q] = cvtpk(v0, v1); pk[2 * q + 1] = cvtpk(v2, v3); }
                char* dst = P.ws + (isk ? WS_K : WS_Q);
#pragma unroll
                for (int hd = 0; hd < 2; ++hd) st16_sc1(dst, (unsigned)(((hd0 + hd) * 64 + tile) * 64 + lane) * 16, (u32x4){pk[4 * hd], pk[4 * hd + 1], pk[4 * hd + 2], pk[4 * hd + 3]});
                SBAR();
            }
        }
        {
            const float csvv = sc[128 + 128 + hf * 32 + r];
            unsigned pk[8];
#pragma unroll
            for (int q = 0; q < 4; ++q) { const f32x4 s0 = *(const f32x4*)&sc[(32 * rt + 8 * q + 4 * hh) * 2], s1 = *(const f32x4*)&sc[(32 * rt + 8 * q + 4 * hh) * 2 + 4];
                const float v0 = (accV[4 * q] - s0[0] * csvv) * s0[1], v1 = (accV[4 * q + 1] - s0[2] * csvv) * s0[3], v2 = (accV[4 * q + 2] - s1[0] * csvv) * s1[1], v3 = (accV[4 * q + 3] - s1[2] * csvv) * s1[3];
                pk[2 * q] = cvtpk(v0, v1); pk[2 * q + 1] = cvtpk(v2, v3); }
            const int head = hd0 + (r >> 4), d = r & 15;
#pragma unroll
            for (int s = 0; s < 2; ++s) st16_sc1(P.ws + WS_V, (unsigned)((((head * 128 + tile * 2 + s) * 2 + hh) * 16 + d) * 16), (u32x4){pk[4 * s], pk[4 * s + 1], pk[4 * s + 2], pk[4 * s + 3]});
        }
    }
    if constexpr (FUSED) { publish(P.ws, FL_Q + cg * 32 + rb, tag); if (threadIdx.x == 0) flag_store(P.ws, FL_START + blockIdx.x, tag); } else { WAITV(0); BARRIER(); }
    STAMP(3);
}

constexpr int G_ST = 5, G_AB = 8192, G_SB = 16384;
template <bool FUSED>
__device__ __forceinline__ void phase_u(const Params& P, char* lds, u64 tag) {
    const int tid = threadIdx.x, lane = tid & 63, r = lane & 31, hh = lane >> 5;
    const int w = __builtin_amdgcn_readfirstlane(tid >> 6), wm = (w >> 1) & 1, wn = w & 1;
    const int b = blockIdx.x & 255, xcd = b & 7, bi = b >> 3;
    const bool isA = blockIdx.x >= 256;
    if (!isA && bi >= 24) return;
    constexpr int ntile = 1;
    const int rb = 8 * (xcd & 1) + (bi < 24 ? 2 * (bi & 3) + (isA ? 0 : 1) : bi - 24), cb0 = 7 * (xcd >> 1) + (bi < 24 ? bi >> 2 : 6);
    const int m0 = rb * 128;
    const unsigned lds0 = (unsigned)(uintptr_t)lds;
    unsigned pf = 0;
    if (isA && w >= 4) {
        const int gw = bi * 4 + (w & 3), li = (gw < 112 ? gw : 0) * 64 + lane, m = li / 3584, l2 = li % 3584, row = l2 / 7, c = l2 % 7;
        const char* a = (m ? P.wu : P.wg) + (size_t)row * (DF * 2) + ((xcd >> 1) * 7 + c) * 128;
        asm volatile("global_load_dword %0, %1, off" : "=v"(pf) : "v"(a) : "memory");
    }
    wait_flags<FUSED>(P.ws, FL_S + 16 * rb, 16, tag);
    STAMP(isA ? 8 : 4);
    if (w >= 4) {
        const int p = w & 3;
        unsigned woff;
        { const int row = 8 * p + (lane >> 3), ch = (lane & 7) ^ (((row >> 1) & 1) << 2); woff = row * (DF * 2) + cb0 * 128 + ch * 16; }
        const char* ag = uni(P.ws + WS_XN + (size_t)m0 * 64 + p * 2048);
        const char* wgp = uni(P.wg); const char* wup = uni(P.wu);
        const unsigned ldsA = uni(lds0 + 2 * p * 1024), ldsW = uni(lds0 + G_AB + p * 1024);
        auto issue = [&](int st, int slot) { const int kt = st & 15; const int wo = kt * (32 * DF * 2) + (st >> 4) * 512;
            gldsN<true, 2>(ag + kt * (2048 * 64), ldsA + slot * G_SB, lane * 16, lane * 16);
            glds16<false>(wgp + wo, woff, ldsW + slot * G_SB); glds16<false>(wup + wo, woff, ldsW + 4096 + slot * G_SB); };
#pragma unroll
        for (int i = 0; i < G_ST; ++i) issue(i, i);
        loader_loop_rt<G_ST, 4>(16 * ntile, issue);
        asm volatile("s_waitcnt vmcnt(0)" : "+v"(pf) :: "memory");
    } else {
        f32x16 acc[2][2] = {};
        unsigned a_rd[2];
        { const int arow = 64 * wm + r; a_rd[0] = arow * 64 + ((0 + hh) ^ ((arow >> 2) & 3)) * 16; a_rd[1] = arow * 64 + ((2 + hh) ^ ((arow >> 2) & 3)) * 16; }
        const int q4 = (lane & 15) >> 2;
        const unsigned chg = (4 * wn + 2 * ((lane >> 4) & 1) + ((lane & 3) >> 1)) ^ (((q4 >> 1) & 1) << 2);
        const unsigned w_rd = G_AB + (8 * hh + q4) * 128 + chg * 16 + 8 * (lane & 1);
        auto epilogue = [&](int cb) {
            constexpr float GA = -2.f * 0.7978845608028654f * 1.4426950408889634f, GB = GA * 0.044715f;
#pragma unroll
            for (int i = 0; i < 2; ++i) {
                unsigned pk[8];
#pragma unroll
                for (int q = 0; q < 4; ++q) {
                    float hv[4];
#pragma unroll
                    for (int j = 0; j < 4; ++j) {
                        const float gv = acc[i][0][4 * q + j], uv = acc[i][1][4 * q + j];
                        const float z = gv * (GA + GB * gv * gv);
                        hv[j] = gv * uv * __builtin_amdgcn_rcpf(1.f + __builtin_amdgcn_exp2f(z));
                    }
                    pk[2 * q] = cvtpk(hv[0], hv[1]); pk[2 * q + 1] = cvtpk(hv[2], hv[3]);
                }
                const unsigned off = (unsigned)(((rb * 4 + 2 * wm + i) * 112 + cb * 4 + 2 * wn) * 64 + lane) * 16;
                st16_sc1(P.ws + WS_H, off, (u32x4){pk[0], pk[1], pk[2], pk[3]}); st16_sc1(P.ws + WS_H, off + 1024, (u32x4){pk[4], pk[5], pk[6], pk[7]});
            } };
        gemm_loop_rt<G_ST, 0, G_SB, 4, 6, -1, Frag4>(16 * ntile, [&](int, int) {},
            [&](Frag4& f, int so, int kk, int) { lds_cptr sb = (lds_cptr)lds + so;
                f.c = ldsr128(sb + a_rd[kk]);
                f.a = cat8(vtr(sb + w_rd + kk * 2048), vtr(sb + w_rd + kk * 2048 + 512));
                f.b = cat8(vtr(sb + w_rd + 4096 + kk * 2048), vtr(sb + w_rd + 4096 + kk * 2048 + 512));
                f.d = ldsr128(sb + a_rd[kk] + 2048); },
            [&](const Frag4& f, int) {
                acc[0][0] = MFMA32(f.a, f.c, acc[0][0]); acc[0][1] = MFMA32(f.b, f.c, acc[0][1]);
                acc[1][0] = MFMA32(f.a, f.d, acc[1][0]); acc[1][1] = MFMA32(f.b, f.d, acc[1][1]); PIN(acc[0][0]); PIN(acc[0][1]); PIN(acc[1][0]); PIN(acc[1][1]); },
            [&](int s) { if (s == 15) { epilogue(cb0); acc[0][0] = (f32x16){}; acc[0][1] = (f32x16){}; acc[1][0] = (f32x16){}; acc[1][1] = (f32x16){}; SBAR(); } });
        STAMP(isA ? 9 : 5);
        epilogue(cb0 + 4 * (ntile - 1));
    }
    if constexpr (FUSED) {
        if (isA) { BARRIER(); }
        else { WAITV(0); BARRIER(); if (threadIdx.x == 0) flag_store(P.ws, FL_H + rb * 32 + cb0, tag); }
    } else { WAITV(0); BARRIER(); }
    STAMP(isA ? 10 : 6);
}

constexpr int D_ST = 4, D_AB = 8192, D_SB = 16384;
template <bool FUSED>
__device__ __forceinline__ void phase_d(const Params& P, char* lds, u64 tag) {
    const int tid = threadIdx.x, lane = tid & 63, r = lane & 31, hh = lane >> 5;
    const int w = __builtin_amdgcn_readfirstlane(tid >> 6), wk = w >> 2, wm = (w >> 1) & 1, wn = w & 1;
    const int b = blockIdx.x & 255, xcd = b & 7, bi = b >> 3, rb = xcd * 4 + (bi & 3), cb = bi >> 2;
    const int m0 = rb * 64, n0 = cb * 64;
    const int kt0 = 0;
    const unsigned lds0 = (unsigned)(uintptr_t)lds;
    constexpr int NT = 28;
    const int row = m0 + 32 * wm + r;
    u32x2 xv[2];
#pragma unroll
    for (int q = 0; q < 2; ++q) xv[q] = *(const u32x2*)(P.x + (size_t)row * 1024 + (n0 + 32 * wn + 4 * hh + 8 * (2 * wk + q)) * 2);
    const int q4 = (lane & 15) >> 2;
    const unsigned chw = (4 * wn + 2 * ((lane >> 4) & 1) + ((lane & 3) >> 1)) ^ (((q4 >> 1) & 1) << 2);
    const unsigned w_rd = (4 * hh + q4) * 128 + chw * 16 + 8 * (lane & 1);
    f32x16 acc = {};
    if (wk == 1) {
        const int p = w & 3;
        unsigned aoff[2], woff2[2];
#pragma unroll
        for (int i = 0; i < 2; ++i) { const int rw = 8 * (2 * p + i) + (lane >> 3); aoff[i] = lane * 16;
            const int ch = (lane & 7) ^ (((rw >> 1) & 1) << 2); woff2[i] = rw * 1024 + n0 * 2 + ch * 16 - 1024 * i; }
        const char* hg = uni(P.ws + WS_H + (size_t)((rb * 2 + (p >> 1)) * 112 + 2 * (p & 1)) * 1024); const char* wd = uni(P.wd);
        const unsigned ldsA = uni(lds0 + 2 * p * 1024), ldsW = uni(lds0 + D_AB + 2 * p * 1024);
        auto issue_w = [&](int it, int slot) { int kt = kt0 + it; if (kt >= NT) kt -= NT; gldsN<false, 2>(wd + kt * (64 * 1024), ldsW + slot * D_SB, woff2[0], woff2[1]); };
        auto issue_a = [&](int it, int slot) { int kt = kt0 + it; if (kt >= NT) kt -= NT; gldsN<true, 2>(hg + kt * 4096, ldsA + slot * D_SB, aoff[0], aoff[1]); };
#pragma unroll
        for (int i = 0; i < D_ST; ++i) issue_w(i, i);
        wait_flags<FUSED>(P.ws, FL_H + (rb >> 1) * 32, 28, tag);
#pragma unroll
        for (int i = 0; i < D_ST; ++i) { issue_a(i, i); issue_w(i, i); }
        loader_loop<NT, D_ST, 4>([&](int it, int slot) { issue_a(it, slot); issue_w(it, slot); });
    } else {
        wait_flags<FUSED>(P.ws, FL_H + (rb >> 1) * 32, 28, tag);
        STAMP(7);
        const int arow = 32 * wm + r;
        unsigned a_rd[4];
#pragma unroll
        for (int kk = 0; kk < 4; ++kk) a_rd[kk] = wm * 4096 + kk * 1024 + lane * 16;
        struct FragD { Frag4 k0, k1; };
        auto rd = [&](Frag4& f, lds_cptr sb, int k) {
            f.a = cat8(vtr(sb + D_AB + w_rd + (2 * k) * 2048), vtr(sb + D_AB + w_rd + (2 * k) * 2048 + 1024));
            f.b = ldsr128(sb + a_rd[2 * k]);
            f.c = cat8(vtr(sb + D_AB + w_rd + (2 * k + 1) * 2048), vtr(sb + D_AB + w_rd + (2 * k + 1) * 2048 + 1024));
            f.d = ldsr128(sb + a_rd[2 * k + 1]); };
        cons_loop<NT, D_ST, D_SB, 4, 12, 0, FragD>(
            [&](FragD& f, int so, int) { lds_cptr sb = (lds_cptr)lds + so; rd(f.k0, sb, 0); rd(f.k1, sb, 1); },
            [&](const FragD& f) { acc = MFMA32(f.k0.a, f.k0.b, acc); acc = MFMA32(f.k0.c, f.k0.d, acc); acc = MFMA32(f.k1.a, f.k1.b, acc); acc = MFMA32(f.k1.c, f.k1.d, acc); PIN(acc); });
    }
    unsigned woff;
    { const int rw = 8 * w + (lane >> 3); const int ch = (lane & 7) ^ (((rw >> 1) & 1) << 2); woff = rw * 1024 + n0 * 2 + ch * 16; }
    BARRIER();
    STAMP(8);
#pragma unroll
    for (int i = 0; i < 8; ++i) glds16<false>(P.wo + (size_t)i * 64 * 1024, woff, lds0 + i * 8192 + w * 1024);
    float* ex = (float*)(lds + 65536);
    if (wk == 0) {
#pragma unroll
        for (int j = 0; j < 8; ++j) ex[(w * 8 + j) * 64 + lane] = acc[8 + j];
    }
    BARRIER();
    float gate[8];
#pragma unroll
    for (int j = 0; j < 8; ++j) { const float yv = wk ? ex[((w ^ 4) * 8 + j) * 64 + lane] : acc[j]; gate[j] = yv * __builtin_amdgcn_rcpf(1.f + __builtin_amdgcn_exp2f(-1.4426950408889634f * yv)); }
    if constexpr (FUSED) {
        if (blockIdx.x == 0 && w == 7) {
            bool ok = false;
            for (unsigned spin = 0; spin < (1u << 16) && !ok; ++spin) { bool a = true;
#pragma unroll
                for (int i = 0; i < 8; ++i) a &= flag_load(P.ws, FL_START + i * 64 + lane) == tag;
                ok = __all(a); if (!ok) __builtin_amdgcn_s_sleep(8); }
            if (lane == 0) flag_store(P.ws, FL_GEN, tag);
        }
    }
    WAITV(0);
    wait_flags<FUSED>(P.ws, FL_O + (rb >> 2) * 32, 32, tag);
    STAMP(9);
    u32x4 of[16];
    {
        const char* og = P.ws + WS_O + ((size_t)(rb * 2 + wm) * 32 + 16 * wk) * 1024;
#pragma unroll
        for (int i = 0; i < 16; ++i) asm volatile("s_nop 4\n\tglobal_load_dwordx4 %0, %1, %2 offset:%3 sc1" : "=v"(of[i]) : "v"(lane * 16 + (i >> 2) * 4096), "s"(uni(og)), "n"((i & 3) * 1024) : "memory");
    }
    f32x16 accS = {};
#pragma unroll
    for (int i = 0; i < 16; ++i) {
        const bf16x8 wb = cat8(vtr((lds_cptr)lds + w_rd + (16 * wk + i) * 2048), vtr((lds_cptr)lds + w_rd + (16 * wk + i) * 2048 + 1024));
        if (i == 0) asm volatile("s_waitcnt vmcnt(12)" : "+v"(of[0]), "+v"(of[1]), "+v"(of[2]), "+v"(of[3]) :: "memory");
        if (i == 4) asm volatile("s_waitcnt vmcnt(8)" : "+v"(of[4]), "+v"(of[5]), "+v"(of[6]), "+v"(of[7]) :: "memory");
        if (i == 8) asm volatile("s_waitcnt vmcnt(4)" : "+v"(of[8]), "+v"(of[9]), "+v"(of[10]), "+v"(of[11]) :: "memory");
        if (i == 12) asm volatile("s_waitcnt vmcnt(0)" : "+v"(of[12]), "+v"(of[13]), "+v"(of[14]), "+v"(of[15]) :: "memory");
        accS = MFMA32(wb, __builtin_bit_cast(bf16x8, of[i]), accS);
        if ((i & 1) == 1) SBAR();
    }
#pragma unroll
    for (int j = 0; j < 8; ++j) ex[(w * 8 + j) * 64 + lane] = accS[8 * (1 - wk) + j];
    BARRIER();
    char* orow = P.out + (size_t)row * 1024 + (n0 + 32 * wn + 4 * hh + 16 * wk) * 2;
#pragma unroll
    for (int q = 0; q < 2; ++q) {
        float res[4];
#pragma unroll
        for (int j = 0; j < 4; ++j) {
            const float sv = accS[8 * wk + 4 * q + j] + ex[((w ^ 4) * 8 + 4 * q + j) * 64 + lane];
            const float xx = (j & 1) ? bfhi(xv[q][j >> 1]) : bflo(xv[q][j >> 1]);
            res[j] = xx + sv * gate[4 * q + j];
        }
        *(u32x2*)(orow + q * 16) = (u32x2){cvtpk(res[0], res[1]), cvtpk(res[2], res[3])};
    }
    STAMP(10);
}

constexpr float REF_BIAS = 88.f;
__device__ __forceinline__ void attn_slow(const char* kf, const char* vf, const bf16x8 qf, f32x16& o, int lane) {
    const int r = lane & 31;
    float m = -INFINITY; f32x16 oa = {};
    for (int t = 0; t < 64; ++t) {
        u32x4 kq, va0, va1;
        asm volatile("s_nop 4\n\tglobal_load_dwordx4 %0, %3, %4 sc1\n\tglobal_load_dwordx4 %1, %5, %6 sc1\n\tglobal_load_dwordx4 %2, %5, %6 offset:512 sc1\n\ts_waitcnt vmcnt(0)"
                     : "=&v"(kq), "=&v"(va0), "=&v"(va1) : "v"(t * 1024 + lane * 16), "s"(uni(kf)), "v"(t * 1024 + (lane >> 5) * 256 + (r & 15) * 16), "s"(uni(vf)) : "memory");
        f32x16 s = MFMA32(__builtin_bit_cast(bf16x8, kq), qf, (f32x16){});
        float tm = s[0];
#pragma unroll
        for (int i = 1; i < 16; ++i) tm = fmaxf(tm, s[i]);
        tm = swap_max(tm);
        const float mn = fmaxf(m, tm);
        const float al = __builtin_amdgcn_exp2f(m - mn);
        m = mn;
#pragma unroll
        for (int i = 0; i < 16; ++i) oa[i] *= al;
        unsigned pk[8];
#pragma unroll
        for (int i = 0; i < 8; ++i) pk[i] = cvtpk(__builtin_amdgcn_exp2f(s[2 * i] - mn), __builtin_amdgcn_exp2f(s[2 * i + 1] - mn));
        if (r >= 16) { const unsigned c = (r == 16 || r == 20) ? 0x3f803f80u : 0u; va0 = (u32x4){c, c, c, c}; va1 = va0; }
        oa = MFMA32(__builtin_bit_cast(bf16x8, va0), __builtin_bit_cast(bf16x8, ((u32x4){pk[0], pk[1], pk[2], pk[3]})), oa);
        oa = MFMA32(__builtin_bit_cast(bf16x8, va1), __builtin_bit_cast(bf16x8, ((u32x4){pk[4], pk[5], pk[6], pk[7]})), oa);
    }
    o = oa;
}

constexpr int A_SLOT = 16384, A_CONST = 65536;
template <bool FUSED>
__device__ __forceinline__ void phase_attn(const Params& P, char* lds, u64 tag) {
    const int tid = threadIdx.x, lane = tid & 63, r = lane & 31, hh = lane >> 5;
    const int w = __builtin_amdgcn_readfirstlane(tid >> 6);
    const int b = blockIdx.x & 255, xcd = b & 7, bi = b >> 3, head = xcd * 4 + (bi & 3), qb = bi >> 2;
    const unsigned lds0 = (unsigned)(uintptr_t)lds;
    const char* kf = P.ws + WS_K + (size_t)head * 65536; const char* vf = P.ws + WS_V + (size_t)head * 65536;
    const int hflag = FL_H + (8 * (xcd & 1) + (bi < 24 ? 2 * (bi & 3) : bi - 24)) * 32 + 7 * (xcd >> 1) + (bi < 24 ? bi >> 2 : 6);
    { const int row = tid & 15; const unsigned c = (row == 0 || row == 4) ? 0x3f803f80u : 0u; *(u32x4*)(lds + A_CONST + tid * 16) = (u32x4){c, c, c, c}; }
    wait_flags<FUSED>(P.ws, FL_Q + (head >> 2) * 32, 32, tag);
    if constexpr (FUSED) { if (threadIdx.x == 0) flag_store(P.ws, FL_START + blockIdx.x, tag); }
    STAMP(1);
    auto issue = [&](int ci) {
        const int c = (qb + ci) & 7; const unsigned sb = lds0 + (ci & 3) * A_SLOT;
        glds16<true>(kf + c * 8192 + w * 1024, lane * 16, sb + w * 1024);
        glds16<true>(vf + c * 8192 + w * 1024, lane * 16, sb + 8192 + w * 1024);
    };
    issue(0);
    u32x4 qv;
    asm volatile("s_nop 4\n\tglobal_load_dwordx4 %0, %1, %2 sc1" : "=v"(qv) : "v"(lane * 16), "s"(uni(P.ws + WS_Q + (size_t)(head * 64 + qb * 8 + w) * 1024)) : "memory");
    issue(1); issue(2); issue(3);
    asm volatile("s_waitcnt vmcnt(6)" : "+v"(qv) :: "memory");
    BARRIER();
    if constexpr (FUSED) { if (threadIdx.x == 0) flag_store(P.ws, hflag, tag); }
    STAMP(2);
    const bf16x8 qf = __builtin_bit_cast(bf16x8, qv);
    const unsigned kaddr = lane * 16;
    const unsigned vaddr = (r < 16) ? (8192 + hh * 256 + r * 16) : (A_CONST + hh * 256 + (r - 16) * 16);
    const unsigned vmul = (r < 16) ? 1u : 0u;
    f32x16 o, cneg, sA, sB;
    {
        const bf16x8 k0 = ldsr128((lds_cptr)lds + kaddr), k1 = ldsr128((lds_cptr)lds + kaddr + 1024);
        const f32x16 s0 = MFMA32(k0, qf, (f32x16){}), s1 = MFMA32(k1, qf, (f32x16){});
        float tm = fmaxf(s0[0], s1[0]);
#pragma unroll
        for (int i = 1; i < 16; ++i) tm = fmaxf(tm, fmaxf(s0[i], s1[i]));
        tm = swap_max(tm);
        const float c = -(tm + REF_BIAS);
#pragma unroll
        for (int i = 0; i < 16; ++i) { cneg[i] = c; o[i] = 0.f; }
        PIN(cneg);
        sA = MFMA32(k0, qf, cneg);
    }
    lds_cptr kp = (lds_cptr)lds + kaddr;
    lds_cptr vp = (lds_cptr)lds + vaddr;
    bf16x8 kfr = ldsr128(kp + 1024);
    bf16x8 v0f = ldsr128(vp), v1f;
    { u32x4 z = {0u, 0u, 0u, 0u}; v1f = __builtin_bit_cast(bf16x8, z); }
    u32x4 pk0 = {0u, 0u, 0u, 0u}, pk1 = {0u, 0u, 0u, 0u};
#define EXP2(x) __builtin_amdgcn_exp2f(x)
#define EX(dst, src) do { dst = EXP2(src); PIN(dst); } while (0)
#define CV(dst, a, b) do { dst = cvtpk(a, b); PIN(dst); } while (0)
#define BF(x) __builtin_bit_cast(bf16x8, x)
#define STEP(cur, nxt, KN, V1, V0N) do { \
        float e[16]; \
        nxt = MFMA32(kfr, qf, cneg); PIN(nxt); SBAR(); \
        PIN(cur); kfr = ldsr128(KN); SBAR(); \
        EX(e[0], cur[0]); EX(e[1], cur[1]); EX(e[2], cur[2]); EX(e[3], cur[3]); EX(e[4], cur[4]); EX(e[5], cur[5]); EX(e[6], cur[6]); SBAR(); \
        o = MFMA32(v1f, BF(pk1), o); PIN(o); SBAR(); \
        PIN(cur); PIN(e[0]); PIN(e[2]); PIN(e[4]); PIN(e[6]); v1f = ldsr128(V1); SBAR(); \
        EX(e[7], cur[7]); CV(pk0[0], e[0], e[1]); CV(pk0[1], e[2], e[3]); CV(pk0[2], e[4], e[5]); \
        EX(e[8], cur[8]); CV(pk0[3], e[6], e[7]); EX(e[9], cur[9]); EX(e[10], cur[10]); EX(e[11], cur[11]); SBAR(); \
        o = MFMA32(v0f, BF(pk0), o); PIN(o); SBAR(); \
        PIN(cur); PIN(e[8]); PIN(e[10]); v0f = ldsr128(V0N); SBAR(); \
        EX(e[12], cur[12]); EX(e[13], cur[13]); EX(e[14], cur[14]); EX(e[15], cur[15]); \
        CV(pk1[0], e[8], e[9]); CV(pk1[1], e[10], e[11]); CV(pk1[2], e[12], e[13]); CV(pk1[3], e[14], e[15]); SBAR(); \
    } while (0)
    __builtin_amdgcn_s_setprio(APRIO);
    for (int ci = 0; ci < 8; ++ci) {
        lds_cptr kc = kp + (ci & 3) * A_SLOT, kn = kp + ((ci + 1) & 3) * A_SLOT;
        lds_cptr vc = vp + vmul * ((ci & 3) * A_SLOT), vn = vp + vmul * (((ci + 1) & 3) * A_SLOT);
        STEP(sA, sB, kc + 2 * 1024, vc + 0 * 1024 + 512, vc + 1 * 1024);
        STEP(sB, sA, kc + 3 * 1024, vc + 1 * 1024 + 512, vc + 2 * 1024);
        STEP(sA, sB, kc + 4 * 1024, vc + 2 * 1024 + 512, vc + 3 * 1024);
        STEP(sB, sA, kc + 5 * 1024, vc + 3 * 1024 + 512, vc + 4 * 1024);
        STEP(sA, sB, kc + 6 * 1024, vc + 4 * 1024 + 512, vc + 5 * 1024);
        STEP(sB, sA, kc + 7 * 1024, vc + 5 * 1024 + 512, vc + 6 * 1024);
        if (ci < 5) WAITV(2); else WAITV(0);
        BARRIER();
        if (ci >= 1 && ci + 3 < 8) issue(ci + 3);
        STEP(sA, sB, kn + 0 * 1024, vc + 6 * 1024 + 512, vc + 7 * 1024);
        STEP(sB, sA, kn + 1 * 1024, vc + 7 * 1024 + 512, vn);
    }
    o = MFMA32(v1f, BF(pk1), o);
    STAMP(3);
    bool bad = !(o[8] < 1.0e37f) || !(o[8] > 0.f);
#pragma unroll
    for (int i = 0; i < 8; ++i) bad |= !(fabsf(o[i]) < 3.0e38f);
    if (__any(bad)) attn_slow(kf, vf, qf, o, lane);
    const float rl = 1.0f / o[8];
    const u32x4 pk = {cvtpk(o[0] * rl, o[1] * rl), cvtpk(o[2] * rl, o[3] * rl), cvtpk(o[4] * rl, o[5] * rl), cvtpk(o[6] * rl, o[7] * rl)};
    st16_sc1(P.ws + WS_O, (unsigned)(((qb * 8 + w) * 32 + head) * 64 + lane) * 16, pk);
    if constexpr (FUSED) publish(P.ws, FL_O + qb * 32 + head, tag);
    STAMP(4);
}

template <bool FUSED>
__device__ __forceinline__ void phase_side(const Params& P, char* lds, u64 tag) {
    const int tid = threadIdx.x, lane = tid & 63;
    const int w = __builtin_amdgcn_readfirstlane(tid >> 6);
    const int j = blockIdx.x & 255;
    const int row = 8 * j + w;
    const unsigned tg = (unsigned)tag;
    const u32x4 xr = *(const u32x4*)(P.x + (size_t)row * 1024 + lane * 16);
    const bool has = j < 96;
    const int cgp = j % 24, sl = j / 24;
    u32x4 wv[2] = {};
    if (has) {
#pragma unroll
        for (int i = 0; i < 2; ++i) wv[i] = *(const u32x4*)(P.wqkv + (size_t)(128 * sl + 16 * w + 8 * i + (lane >> 3)) * 3072 + cgp * 128 + (lane & 7) * 16);
    }
    float v[8];
#pragma unroll
    for (int i = 0; i < 4; ++i) { v[2 * i] = bflo(xr[i]); v[2 * i + 1] = bfhi(xr[i]); }
    float sx = ((v[0] + v[1]) + (v[2] + v[3])) + ((v[4] + v[5]) + (v[6] + v[7]));
    sx = wave_sum(sx);
    STAMP(5);
    const float mu = sx * (1.f / 512.f);
    float sq = 0.f;
#pragma unroll
    for (int i = 0; i < 8; ++i) { v[i] -= mu; sq = fmaf(v[i], v[i], sq); }
    sq = wave_sum(sq);
    const float rstd = __builtin_amdgcn_rsqf(sq * (1.f / 512.f) + 1e-5f);
    if (lane == 0) st16_sc1(P.ws + WS_GST, (unsigned)row * 16, (u32x4){__float_as_uint(mu), tg, __float_as_uint(rstd), tg});
    const u32x4 pk = {cvtpk(v[0] * rstd, v[1] * rstd), cvtpk(v[2] * rstd, v[3] * rstd), cvtpk(v[4] * rstd, v[5] * rstd), cvtpk(v[6] * rstd, v[7] * rstd)};
    st16_sc1(P.ws + WS_XN, (unsigned)((((lane >> 2) * 2048 + row) * 4 + ((lane & 3) ^ ((row >> 2) & 3))) * 16), pk);
    if constexpr (FUSED) publish(P.ws, FL_S + j, tag); else { WAITV(0); BARRIER(); }
    STAMP(7);
    if (has) {
        float a[8];
#pragma unroll
        for (int k = 0; k < 4; ++k) { a[2 * k] = bflo(wv[0][k]) + bflo(wv[1][k]); a[2 * k + 1] = bfhi(wv[0][k]) + bfhi(wv[1][k]); }
#pragma unroll
        for (int k = 0; k < 8; ++k) a[k] = swap_add(swap16_add(dpp_add<0x128>(a[k])));
        float* red = (float*)lds;
        if (lane < 8) { *(f32x4*)&red[w * 64 + lane * 8] = (f32x4){a[0], a[1], a[2], a[3]}; *(f32x4*)&red[w * 64 + lane * 8 + 4] = (f32x4){a[4], a[5], a[6], a[7]}; }
        BARRIER();
        if (tid < 32) {
            f32x2 c = {0.f, 0.f};
#pragma unroll
            for (int i = 0; i < 8; ++i) c += *(const f32x2*)&red[i * 64 + 2 * tid];
            st16_sc1(P.ws + WS_GCS, (unsigned)(sl * 1536 + cgp * 64 + 2 * tid) * 8, (u32x4){__float_as_uint(c[0]), tg, __float_as_uint(c[1]), tg});
        }
    }
    STAMP(6);
    BARRIER();
}

template <int MASK, bool FUSED>
__global__ __launch_bounds__(512, 4) void mega(Params P) {
    extern __shared__ __attribute__((aligned(16))) char lds[];
    u64 tag = 0;
    if constexpr (FUSED) {
        tag = flag_load(P.ws, FL_GEN) + 1;
    }
    STAMP((MASK & 1) ? 0 : (MASK & 2) ? 11 : (MASK & 8) ? 14 : 12);
    if (blockIdx.x < 256) {
        if constexpr (MASK & 1) phase_q<FUSED>(P, lds, tag);
        if constexpr (MASK & 2) phase_u<FUSED>(P, lds, tag);
        if constexpr (MASK & 4) { BARRIER(); phase_d<FUSED>(P, lds, tag); }
    } else {
        if constexpr (MASK & 1) phase_side<FUSED>(P, lds, tag);
        if constexpr (MASK & 2) phase_u<FUSED>(P, lds, tag);
        if constexpr (MASK & 8) phase_attn<FUSED>(P, lds, tag);
    }
}

extern "C" void kernel_launch(void* const* d_in, const int* in_sizes, int n_in, void* d_out, int out_size, void* d_ws, size_t ws_size, hipStream_t stream) {
    Params P;
    P.x = (const char*)d_in[0]; P.wqkv = (const char*)d_in[1]; P.wo = (const char*)d_in[2];
    P.wg = (const char*)d_in[4]; P.wu = (const char*)d_in[5]; P.wd = (const char*)d_in[6];
    P.out = (char*)d_out; P.ws = (char*)d_ws;
    (void)hipFuncSetAttribute((const void*)mega<15, true>, hipFuncAttributeMaxDynamicSharedMemorySize, LDS_BYTES);
    mega<15, true><<<512, 512, LDS_BYTES, stream>>>(P);
}
```

```cpp
#include <hip/hip_runtime.h>
#include <hip/hip_bf16.h>
#include <stdint.h>

typedef short bf16x8 __attribute__((ext_vector_type(8)));
typedef short s16x4 __attribute__((ext_vector_type(4)));
typedef short v4i16_t __attribute__((ext_vector_type(4)));
typedef float f32x16 __attribute__((ext_vector_type(16)));
typedef float f32x4 __attribute__((ext_vector_type(4)));
typedef float f32x2 __attribute__((ext_vector_type(2)));
typedef unsigned u32x4 __attribute__((ext_vector_type(4)));
typedef unsigned u32x2 __attribute__((ext_vector_type(2)));
typedef __bf16 bf2_t __attribute__((ext_vector_type(2)));
typedef unsigned long long u64;
#define LDSP __attribute__((address_space(3)))
#define GLBP __attribute__((address_space(1)))
typedef LDSP const char* lds_cptr;
typedef LDSP char* lds_ptr;

constexpr int SEQ = 2048, DM = 512, DF = 1792, NH = 32;
constexpr size_t WS_Q = 0, WS_K = 2u << 20, WS_V = 4u << 20, WS_O = 6u << 20, WS_H = 8u << 20, WS_STATS = 16u << 20, WS_FLAGS = (16u << 20) + 131072;
constexpr int FL_GEN = 0, FL_START = 16, FL_Q = 16 + 512, FL_H = FL_Q + 256, FL_O = FL_H + 512, FL_S = FL_O + 256, FL_QL = FL_S + 256, FL_END = FL_QL + 256;
constexpr size_t WS_GST = (16u << 20), WS_GCS = (16u << 20) + 32768;
constexpr size_t WS_XN = 18u << 20;
constexpr float QSCALE = 0.25f * 1.4426950408889634f;
constexpr int LDS_BYTES = 81920;

#define MFMA32(a, b, c) __builtin_amdgcn_mfma_f32_32x32x16_bf16((a), (b), (c), 0, 0, 0)
#define SBAR() __builtin_amdgcn_sched_barrier(0)
#define PIN(x) asm volatile("" : "+v"(x))

__device__ __forceinline__ unsigned cvtpk(float lo, float hi) { return __builtin_bit_cast(unsigned, __builtin_convertvector(((f32x2){lo, hi}), bf2_t)); }
__device__ __forceinline__ unsigned uni(unsigned v) { return (unsigned)__builtin_amdgcn_readfirstlane((int)v); }
template <class T> __device__ __forceinline__ T* uni(T* p) { const u64 v = (u64)p; return (T*)(((u64)uni((unsigned)(v >> 32)) << 32) | uni((unsigned)v)); }
#define GLDS_ASM(SC) \
    if constexpr (NP == 1) asm volatile("s_mov_b32 m0, %2\n\ts_nop 0\n\tglobal_load_lds_dwordx4 %0, %1" SC :: "v"(v0), "s"(gbase), "s"(lds_base) : "memory"); \
    else if constexpr (NP == 2) asm volatile("s_mov_b32 m0, %3\n\ts_nop 0\n\tglobal_load_lds_dwordx4 %0, %2" SC "\n\tglobal_load_lds_dwordx4 %1, %2 offset:1024" SC :: "v"(v0), "v"(v1), "s"(gbase), "s"(lds_base) : "memory"); \
    else if constexpr (NP == 3) asm volatile("s_mov_b32 m0, %4\n\ts_nop 0\n\tglobal_load_lds_dwordx4 %0, %3" SC "\n\tglobal_load_lds_dwordx4 %1, %3 offset:1024" SC "\n\tglobal_load_lds_dwordx4 %2, %3 offset:2048" SC :: "v"(v0), "v"(v1), "v"(v2), "s"(gbase), "s"(lds_base) : "memory"); \
    else asm volatile("s_mov_b32 m0, %5\n\ts_nop 0\n\tglobal_load_lds_dwordx4 %0, %4" SC "\n\tglobal_load_lds_dwordx4 %1, %4 offset:1024" SC "\n\tglobal_load_lds_dwordx4 %2, %4 offset:2048" SC "\n\tglobal_load_lds_dwordx4 %3, %4 offset:3072" SC :: "v"(v0), "v"(v1), "v"(v2), "v"(v3), "s"(gbase), "s"(lds_base) : "memory");
template <bool SC1, int NP>
__device__ __forceinline__ void gldsN(const void* gbase, unsigned lds_base, unsigned v0, unsigned v1 = 0, unsigned v2 = 0, unsigned v3 = 0) {
    if constexpr (SC1) { GLDS_ASM(" sc1") } else { GLDS_ASM("") }
}
template <bool SC1>
__device__ __forceinline__ void glds16(const void* gbase, unsigned voff, unsigned lds_base) { gldsN<SC1, 1>(gbase, lds_base, voff); }
__device__ __forceinline__ void st16_sc1(void* gbase, unsigned voff, u32x4 v) { asm volatile("s_nop 4\n\tglobal_store_dwordx4 %0, %1, %2 sc1\n\ts_nop 1" :: "v"(voff), "v"(v), "s"(uni(gbase)) : "memory"); }
__device__ __forceinline__ void st8_sc1(void* gbase, unsigned voff, f32x2 v) { asm volatile("s_nop 4\n\tglobal_store_dwordx2 %0, %1, %2 sc1\n\ts_nop 1" :: "v"(voff), "v"(v), "s"(uni(gbase)) : "memory"); }
__device__ __forceinline__ bf16x8 ldsr128(lds_cptr p) { return *(const LDSP bf16x8*)p; }
__device__ __forceinline__ s16x4 vtr(lds_cptr p) { return __builtin_bit_cast(s16x4, __builtin_amdgcn_ds_read_tr16_b64_v4i16((LDSP v4i16_t*)p)); }
__device__ __forceinline__ bf16x8 cat8(s16x4 a, s16x4 b) { return (bf16x8){a[0], a[1], a[2], a[3], b[0], b[1], b[2], b[3]}; }
__device__ __forceinline__ float dot2(unsigned a, unsigned b, float c) { return __builtin_amdgcn_fdot2_f32_bf16(__builtin_bit_cast(bf2_t, a), __builtin_bit_cast(bf2_t, b), c, false); }
__device__ __forceinline__ float swap_add(float v) {
    auto rr = __builtin_amdgcn_permlane32_swap(__float_as_uint(v), __float_as_uint(v), false, false);
    return __uint_as_float(rr[0]) + __uint_as_float(rr[1]);
}
__device__ __forceinline__ float swap_max(float v) {
    auto rr = __builtin_amdgcn_permlane32_swap(__float_as_uint(v), __float_as_uint(v), false, false);
    return __builtin_fmaxf(__uint_as_float(rr[0]), __uint_as_float(rr[1]));
}
template <int CTRL> __device__ __forceinline__ float dpp_add(float v) { return v + __int_as_float(__builtin_amdgcn_update_dpp(0, __float_as_int(v), CTRL, 0xf, 0xf, true)); }
__device__ __forceinline__ float swap16_add(float v) {
    auto rr = __builtin_amdgcn_permlane16_swap(__float_as_uint(v), __float_as_uint(v), false, false);
    return __uint_as_float(rr[0]) + __uint_as_float(rr[1]);
}
__device__ __forceinline__ float wave_sum(float v) {
    v = dpp_add<0xB1>(v); v = dpp_add<0x4E>(v); v = dpp_add<0x141>(v); v = dpp_add<0x140>(v);
    return swap_add(swap16_add(v));
}
#define WAITV(n) asm volatile("s_waitcnt vmcnt(" #n ")" ::: "memory")
#define BARRIER() asm volatile("s_waitcnt lgkmcnt(0)\n\ts_barrier" ::: "memory")
__device__ __forceinline__ float bflo(unsigned u) { return __uint_as_float(u << 16); }
__device__ __forceinline__ float bfhi(unsigned u) { return __uint_as_float(u & 0xffff0000u); }


constexpr int TAG_LDS = 81920 - 128;
#define TAG_READ() (*(const volatile LDSP u64*)((lds_cptr)lds + TAG_LDS + (threadIdx.x >> 6) * 16) + 1)
#define TAG_READY() do { if constexpr (FUSED) { tag = TAG_READ(); } } while (0)
#define TAG_READY_AFTER(x) do { if constexpr (FUSED) { asm volatile("" : "+v"(x) :: "memory"); tag = TAG_READ(); } } while (0)
struct Params { const char* x; const char* wqkv; const char* wo; const char* wg; const char* wu; const char* wd; char* out; char* ws; };

__device__ __forceinline__ u64 flag_load(const char* ws, int idx) { return __hip_atomic_load((const GLBP u64*)(ws + WS_FLAGS) + idx, __ATOMIC_RELAXED, __HIP_MEMORY_SCOPE_AGENT); }
__device__ __forceinline__ void flag_store(char* ws, int idx, u64 v) { __hip_atomic_store((GLBP u64*)(ws + WS_FLAGS) + idx, v, __ATOMIC_RELAXED, __HIP_MEMORY_SCOPE_AGENT); }
__device__ __forceinline__ void publish(char* ws, int idx, u64 tag) {
    WAITV(0); BARRIER();
    if (threadIdx.x == 0) flag_store(ws, idx, tag);
}
template <bool WAIT>
__device__ __forceinline__ void wait_flags(char* ws, int idx, int n, u64 tag) {
    if constexpr (WAIT) {
        if (threadIdx.x < 64) {
            const int i = idx + ((int)threadIdx.x < n ? (int)threadIdx.x : 0);
            for (unsigned spin = 0; spin < (1u << 16); ++spin) {
                const u64 v = flag_load(ws, i);
                if (__all(v == tag)) break;
                __builtin_amdgcn_s_sleep(2);
            }
        }
    }
    BARRIER();
}


__device__ __forceinline__ void waitv_n(int n) {
    if (n <= 0) WAITV(0); else if (n == 1) WAITV(1); else if (n == 2) WAITV(2); else if (n == 3) WAITV(3); else if (n == 4) WAITV(4); else if (n == 5) WAITV(5);
    else if (n == 6) WAITV(6); else if (n == 7) WAITV(7); else if (n == 8) WAITV(8); else if (n == 9) WAITV(9); else WAITV(10);
}
#define SGB(mask, n) __builtin_amdgcn_sched_group_barrier(mask, n, 0)
template <int NM, int NR, int NV>
__device__ __forceinline__ void interleave() {
    if constexpr (NV < 0) {
#pragma unroll
        for (int i = 0; i < NM; ++i) { SGB(0x8, 1); if (3 * i < NR) SGB(0x100, 3); }
    } else {
#pragma unroll
        for (int i = 0; i < NM; ++i) { SGB(0x8, 1); SGB(0x100, (NR + NM - 1) / NM); if (NV) SGB(0x2, (NV + NM - 1) / NM); }
    }
}
template <int D, int NPW, int SB, int NM, int NR, int NV, class Frag, class Issue, class Read, class Mma, class After>
__device__ __forceinline__ void gemm_loop_rt(const int N, Issue issue, Read read, Mma mma, After after) {
    Frag f0, f1;
    if constexpr (NPW > 0) waitv_n((D - 1) * NPW);
    BARRIER();
    read(f0, 0, 0, 0);
    int rs = 0;
#pragma nounroll
    for (int s = 0; s < N - D; ++s) {
        const int ns = rs == D - 1 ? 0 : rs + 1;
        read(f1, rs * SB, 1, s); mma(f0, 0); interleave<NM, NR, NV>(); SBAR();
        if constexpr (NPW > 0) waitv_n((D - 2) * NPW);
        BARRIER();
        issue(s + D, rs); SBAR();
        read(f0, ns * SB, 0, s + 1); mma(f1, 1); interleave<NM, NR, NV>(); SBAR();
        after(s);
        rs = ns;
    }
#pragma unroll
    for (int t = 0; t < D; ++t) {
        const int ns = rs == D - 1 ? 0 : rs + 1;
        read(f1, rs * SB, 1, N - D + t); mma(f0, 0); interleave<NM, NR, NV>(); SBAR();
        if (t < D - 1) { if constexpr (NPW > 0) waitv_n((D - 2 - t) * NPW); BARRIER(); read(f0, ns * SB, 0, N - D + t + 1); }
        mma(f1, 1); if (t < D - 1) interleave<NM, NR, NV>(); SBAR();
        rs = ns;
    }
}
template <int D, int NPW, class Issue>
__device__ __forceinline__ void loader_loop_rt(const int N, Issue issue) {
    waitv_n((D - 1) * NPW); BARRIER();
    int rs = 0;
#pragma nounroll
    for (int s = 0; s < N - D; ++s) {
        waitv_n((D - 2) * NPW); BARRIER();
        issue(s + D, rs);
        rs = rs == D - 1 ? 0 : rs + 1;
    }
#pragma unroll
    for (int t = 0; t < D - 1; ++t) { waitv_n((D - 2 - t) * NPW); BARRIER(); }
}
template <int N, int D, int SB, int NM, int NR, int NV, class Frag, class Read, class Mma>
__device__ __forceinline__ void cons_loop(Read read, Mma mma) {
    static_assert(N % 2 == 0, "");
    Frag fa, fb;
    BARRIER();
    read(fa, 0, 0);
    int ns = 1;
#pragma nounroll
    for (int s = 0; s < N - 2; s += 2) {
        BARRIER();
        read(fb, ns * SB, s + 1); mma(fa); interleave<NM, NR, NV>(); SBAR();
        ns = ns == D - 1 ? 0 : ns + 1;
        BARRIER();
        read(fa, ns * SB, s + 2); mma(fb); interleave<NM, NR, NV>(); SBAR();
        ns = ns == D - 1 ? 0 : ns + 1;
    }
    BARRIER();
    read(fb, ns * SB, N - 1); mma(fa); interleave<NM, NR, NV>(); SBAR();
    mma(fb); SBAR();
}
template <int N, int D, int NPW, class Issue>
__device__ __forceinline__ void loader_loop(Issue issue) {
    waitv_n((D - 1) * NPW); BARRIER();
    int rs = 0;
#pragma nounroll
    for (int s = 0; s < N - D; ++s) {
        waitv_n((D - 2) * NPW);
        BARRIER();
        issue(s + D, rs);
        rs = rs == D - 1 ? 0 : rs + 1;
    }
#pragma unroll
    for (int t = 0; t < D - 1; ++t) { waitv_n((D - 2 - t) * NPW); BARRIER(); }
}
struct Frag2 { bf16x8 a, b; };
struct Frag3 { bf16x8 a, b, c; };
struct Frag4 { bf16x8 a, b, c, d; };
__device__ __forceinline__ float sum2x4(bf16x8 v, float acc) { const u32x4 u = __builtin_bit_cast(u32x4, v);
    acc = dot2(u[0], 0x3f803f80u, acc); acc = dot2(u[1], 0x3f803f80u, acc); acc = dot2(u[2], 0x3f803f80u, acc); return dot2(u[3], 0x3f803f80u, acc); }
__device__ __forceinline__ float sq2x4(bf16x8 v, float acc) { const u32x4 u = __builtin_bit_cast(u32x4, v);
    acc = dot2(u[0], u[0], acc); acc = dot2(u[1], u[1], acc); acc = dot2(u[2], u[2], acc); return dot2(u[3], u[3], acc); }

constexpr int Q_ST = 4, Q_WB = 12288, Q_A0 = Q_ST * Q_WB, Q_ACH = 8192, Q_SC = Q_A0 + 3 * Q_ACH;
template <bool FUSED>
__device__ __forceinline__ void phase_q(const Params& P, char* lds, u64& tag) {
    const int tid = threadIdx.x, lane = tid & 63, r = lane & 31, hh = lane >> 5;
    const int w = __builtin_amdgcn_readfirstlane(tid >> 6);
    const int b = blockIdx.x & 255, xcd = b & 7, bi = b >> 3;
    const int ri = bi & 7, cj = bi >> 3;
    const int rb = 8 * (2 * (xcd & 1) + ((xcd >> 1) & 1)) + ri, cg = 4 * (xcd >> 2) + cj;
    const int kc0 = 0;
    const int m0 = rb * 64;
    const unsigned lds0 = (unsigned)(uintptr_t)lds;
    float* sc = (float*)(lds + Q_SC);
    if (w >= 4) {
        const int p = w - 4;
        unsigned woff[3];
        unsigned aoff0;
        { const int row = 16 * p + (lane >> 3), c = (lane & 7) ^ ((row >> 1) & 7); aoff0 = row * 1024 + c * 16; }
#pragma unroll
        for (int i = 0; i < 3; ++i) { const int pc = 64 * (3 * p + i) + lane, row = pc / 24, ch = pc % 24, cs = (ch & 7) ^ (((row >> 1) & 1) << 2); woff[i] = row * 3072 + (ch >> 3) * 1024 + cg * 128 + cs * 16; }
        woff[1] -= 1024; woff[2] -= 2048;
        const char* ag = uni(P.x + (size_t)m0 * 1024);
        const char* wq = uni(P.wqkv);
        const unsigned ldsW = uni(lds0 + 3 * p * 1024), ldsX = uni(lds0 + Q_A0 + 2 * p * 1024);
        auto issueW = [&](int st, int slot) { const int kt = (2 * kc0 + st) & 15; gldsN<false, 3>(wq + kt * (32 * 3072), ldsW + slot * Q_WB, woff[0], woff[1], woff[2]); };
        auto issueX = [&](int c, int cslot, int set) { const int kc = (kc0 + c) & 7; glds16<false>(ag + kc * 128 + set * 8192, aoff0 ^ (unsigned)(set << 6), ldsX + cslot * Q_ACH + set * 1024); };
        issueX(0, 0, 0); issueX(0, 0, 1); issueW(0, 0); issueX(1, 1, 0); issueX(1, 1, 1); issueW(1, 1); issueW(2, 2); issueX(2, 2, 0); issueX(2, 2, 1); issueW(3, 3);
        loader_loop<16, Q_ST, 4>([&](int st, int slot) {
            const int u = st + 1; const int c = (u >> 1) < 7 ? (u >> 1) : 7;
            issueW(st, slot); issueX(c, c % 3, u & 1); });
        TAG_READY();
        {
            const unsigned tg = (unsigned)tag;
            if (p == 0) {
                u32x4 v;
                for (unsigned spin = 0; spin < (1u << 16); ++spin) {
                    asm volatile("s_nop 4\n\tglobal_load_dwordx4 %0, %1, %2 sc1\n\ts_waitcnt vmcnt(0)" : "=v"(v) : "v"((unsigned)(m0 + lane) * 16), "s"(uni(P.ws + WS_GST)) : "memory");
                    if (__all(v[1] == tg && v[3] == tg)) break;
                    __builtin_amdgcn_s_sleep(1);
                }
                *(f32x2*)&sc[lane * 2] = (f32x2){__uint_as_float(v[0]), __uint_as_float(v[2])};
            } else {
                u32x4 v0, v1;
                const unsigned off = (unsigned)(((lane >> 5) * 2) * 1536 + (p - 1) * 512 + cg * 64 + 2 * (lane & 31)) * 8;
                for (unsigned spin = 0; spin < (1u << 16); ++spin) {
                    asm volatile("s_nop 4\n\tglobal_load_dwordx4 %0, %2, %3 sc1\n\tglobal_load_dwordx4 %1, %4, %3 sc1\n\ts_waitcnt vmcnt(0)" : "=&v"(v0), "=&v"(v1) : "v"(off), "s"(uni(P.ws + WS_GCS)), "v"(off + 1536 * 8) : "memory");
                    if (__all(v0[1] == tg && v0[3] == tg && v1[1] == tg && v1[3] == tg)) break;
                    __builtin_amdgcn_s_sleep(1);
                }
                const float c0 = swap_add(__uint_as_float(v0[0]) + __uint_as_float(v1[0])), c1 = swap_add(__uint_as_float(v0[2]) + __uint_as_float(v1[2]));
                if (lane < 32) *(f32x2*)&sc[128 + (p - 1) * 64 + 2 * lane] = (f32x2){c0, c1};
            }
        }
        BARRIER();
    } else {
        const int rt = w >> 1, hf = w & 1;
        const int arow = 32 * rt + r;
        unsigned a_rd[2];
#pragma unroll
        for (int k = 0; k < 2; ++k) a_rd[k] = Q_A0 + arow * 128 + ((2 * k + hh) ^ ((arow >> 1) & 7)) * 16;
        const int q4 = (lane & 15) >> 2;
        const unsigned chw = (4 * hf + 2 * ((lane >> 4) & 1) + ((lane & 3) >> 1)) ^ (((q4 >> 1) & 1) << 2);
        const unsigned w_rd = (8 * hh + q4) * 384 + chw * 16 + 8 * (lane & 1);
        unsigned pf0;
        {
            const int idx = w * 64 + lane;
            const char* a;
            if (idx < 144) { const int st = 4 + idx / 12, l = ri * 12 + idx % 12; a = P.wqkv + (size_t)(st * 32 + l / 3) * 3072 + (l % 3) * 1024 + cg * 128; }
            else { const int i2 = idx < 224 ? idx - 144 : 0; a = P.x + (size_t)(m0 + cj * 16 + (i2 & 15)) * 1024 + (3 + (i2 >> 4)) * 128; }
            pf0 = 0;
        }
        f32x16 accQ = {}, accK = {}, accV = {};
        struct FragQ { Frag4 k0, k1; };
        auto rd = [&](Frag4& f, lds_cptr xa, lds_cptr wb) { f.a = ldsr128(xa);
            f.b = cat8(vtr(wb), vtr(wb + 1536)); f.c = cat8(vtr(wb + 128), vtr(wb + 128 + 1536)); f.d = cat8(vtr(wb + 256), vtr(wb + 256 + 1536)); };
        cons_loop<16, Q_ST, Q_WB, 6, 14, 0, FragQ>(
            [&](FragQ& f, int so, int stg) { lds_cptr sb = (lds_cptr)lds + so + w_rd; lds_cptr xb = (lds_cptr)lds + ((stg >> 1) % 3) * Q_ACH; const unsigned fl = (stg & 1) << 6;
                rd(f.k0, xb + (a_rd[0] ^ fl), sb); rd(f.k1, xb + (a_rd[1] ^ fl), sb + 6144); },
            [&](const FragQ& f) {
                accQ = MFMA32(f.k0.b, f.k0.a, accQ); accK = MFMA32(f.k0.c, f.k0.a, accK); accV = MFMA32(f.k0.a, f.k0.d, accV);
                accQ = MFMA32(f.k1.b, f.k1.a, accQ); accK = MFMA32(f.k1.c, f.k1.a, accK); accV = MFMA32(f.k1.a, f.k1.d, accV);
                PIN(accQ); PIN(accK); PIN(accV); });
        asm volatile("s_waitcnt vmcnt(0)" : "+v"(pf0) :: "memory");
        TAG_READY();
        BARRIER(); SBAR();
        const int tile = rb * 2 + rt, hd0 = cg * 4 + hf * 2;
        {
            const f32x2 st = *(const f32x2*)&sc[arow * 2];
            const float mu = st[0], rstd = st[1];
#pragma unroll
            for (int isk = 0; isk < 2; ++isk) {
                const f32x16& acc = isk ? accK : accQ;
                const float scl = isk ? rstd : rstd * QSCALE;
                unsigned pk[8];
#pragma unroll
                for (int q = 0; q < 4; ++q) {
                    const f32x4 c4 = *(const f32x4*)&sc[128 + isk * 64 + hf * 32 + 8 * q + 4 * hh];
                    const float v0 = (acc[4 * q] - mu * c4[0]) * scl, v1 = (acc[4 * q + 1] - mu * c4[1]) * scl, v2 = (acc[4 * q + 2] - mu * c4[2]) * scl, v3 = (acc[4 * q + 3] - mu * c4[3]) * scl;
                    pk[2 * q] = cvtpk(v0, v1); pk[2 * q + 1] = cvtpk(v2, v3); }
                char* dst = P.ws + (isk ? WS_K : WS_Q);
#pragma unroll
                for (int hd = 0; hd < 2; ++hd) st16_sc1(dst, (unsigned)(((hd0 + hd) * 64 + tile) * 64 + lane) * 16, (u32x4){pk[4 * hd], pk[4 * hd + 1], pk[4 * hd + 2], pk[4 * hd + 3]});
                SBAR();
            }
        }
        {
            const float csvv = sc[128 + 128 + hf * 32 + r];
            unsigned pk[8];
#pragma unroll
            for (int q = 0; q < 4; ++q) { const f32x4 s0 = *(const f32x4*)&sc[(32 * rt + 8 * q + 4 * hh) * 2], s1 = *(const f32x4*)&sc[(32 * rt + 8 * q + 4 * hh) * 2 + 4];
                const float v0 = (accV[4 * q] - s0[0] * csvv) * s0[1], v1 = (accV[4 * q + 1] - s0[2] * csvv) * s0[3], v2 = (accV[4 * q + 2] - s1[0] * csvv) * s1[1], v3 = (accV[4 * q + 3] - s1[2] * csvv) * s1[3];
                pk[2 * q] = cvtpk(v0, v1); pk[2 * q + 1] = cvtpk(v2, v3); }
            const int head = hd0 + (r >> 4), d = r & 15;
#pragma unroll
            for (int s = 0; s < 2; ++s) st16_sc1(P.ws + WS_V, (unsigned)((((head * 128 + tile * 2 + s) * 2 + hh) * 16 + d) * 16), (u32x4){pk[4 * s], pk[4 * s + 1], pk[4 * s + 2], pk[4 * s + 3]});
        }
    }
    if constexpr (FUSED) { publish(P.ws, FL_Q + cg * 32 + rb, tag); if (threadIdx.x == 0) flag_store(P.ws, FL_START + blockIdx.x, tag); } else { WAITV(0); BARRIER(); }
}

constexpr int G_ST = 5, G_AB = 8192, G_SB = 16384;
template <bool FUSED>
__device__ __forceinline__ void phase_u(const Params& P, char* lds, u64 tag) {
    const int tid = threadIdx.x, lane = tid & 63, r = lane & 31, hh = lane >> 5;
    const int w = __builtin_amdgcn_readfirstlane(tid >> 6), wm = (w >> 1) & 1, wn = w & 1;
    const int b = blockIdx.x & 255, xcd = b & 7, bi = b >> 3;
    const bool isA = blockIdx.x >= 256;
    if (!isA && bi >= 24) return;
    constexpr int ntile = 1;
    const int rb = 8 * (xcd & 1) + (bi < 24 ? 2 * (bi & 3) + (isA ? 0 : 1) : bi - 24), cb0 = 7 * (xcd >> 1) + (bi < 24 ? bi >> 2 : 6);
    const int m0 = rb * 128;
    const unsigned lds0 = (unsigned)(uintptr_t)lds;
    unsigned pf = 0;
    if (isA && w >= 4) {
        const int gw = bi * 4 + (w & 3), li = (gw < 112 ? gw : 0) * 64 + lane, m = li / 3584, l2 = li % 3584, row = l2 / 7, c = l2 % 7;
        const char* a = (m ? P.wu : P.wg) + (size_t)row * (DF * 2) + ((xcd >> 1) * 7 + c) * 128;
        asm volatile("global_load_dword %0, %1, off" : "=v"(pf) : "v"(a) : "memory");
    }
    wait_flags<FUSED>(P.ws, FL_S + 16 * rb, 16, tag);
    if (w >= 4) {
        const int p = w & 3;
        unsigned woff;
        { const int row = 8 * p + (lane >> 3), ch = (lane & 7) ^ (((row >> 1) & 1) << 2); woff = row * (DF * 2) + cb0 * 128 + ch * 16; }
        const char* ag = uni(P.ws + WS_XN + (size_t)m0 * 64 + p * 2048);
        const char* wgp = uni(P.wg); const char* wup = uni(P.wu);
        const unsigned ldsA = uni(lds0 + 2 * p * 1024), ldsW = uni(lds0 + G_AB + p * 1024);
        auto issue = [&](int st, int slot) { const int kt = st & 15; const int wo = kt * (32 * DF * 2) + (st >> 4) * 512;
            gldsN<true, 2>(ag + kt * (2048 * 64), ldsA + slot * G_SB, lane * 16, lane * 16);
            glds16<false>(wgp + wo, woff, ldsW + slot * G_SB); glds16<false>(wup + wo, woff, ldsW + 4096 + slot * G_SB); };
#pragma unroll
        for (int i = 0; i < G_ST; ++i) issue(i, i);
        loader_loop_rt<G_ST, 4>(16 * ntile, issue);
        asm volatile("s_waitcnt vmcnt(0)" : "+v"(pf) :: "memory");
    } else {
        f32x16 acc[2][2] = {};
        unsigned a_rd[2];
        { const int arow = 64 * wm + r; a_rd[0] = arow * 64 + ((0 + hh) ^ ((arow >> 2) & 3)) * 16; a_rd[1] = arow * 64 + ((2 + hh) ^ ((arow >> 2) & 3)) * 16; }
        const int q4 = (lane & 15) >> 2;
        const unsigned chg = (4 * wn + 2 * ((lane >> 4) & 1) + ((lane & 3) >> 1)) ^ (((q4 >> 1) & 1) << 2);
        const unsigned w_rd = G_AB + (8 * hh + q4) * 128 + chg * 16 + 8 * (lane & 1);
        auto epilogue = [&](int cb) {
            constexpr float GA = -2.f * 0.7978845608028654f * 1.4426950408889634f, GB = GA * 0.044715f;
#pragma unroll
            for (int i = 0; i < 2; ++i) {
                unsigned pk[8];
#pragma unroll
                for (int q = 0; q < 4; ++q) {
                    float hv[4];
#pragma unroll
                    for (int j = 0; j < 4; ++j) {
                        const float gv = acc[i][0][4 * q + j], uv = acc[i][1][4 * q + j];
                        const float z = gv * (GA + GB * gv * gv);
                        hv[j] = gv * uv * __builtin_amdgcn_rcpf(1.f + __builtin_amdgcn_exp2f(z));
                    }
                    pk[2 * q] = cvtpk(hv[0], hv[1]); pk[2 * q + 1] = cvtpk(hv[2], hv[3]);
                }
                const unsigned off = (unsigned)(((rb * 4 + 2 * wm + i) * 112 + cb * 4 + 2 * wn) * 64 + lane) * 16;
                st16_sc1(P.ws + WS_H, off, (u32x4){pk[0], pk[1], pk[2], pk[3]}); st16_sc1(P.ws + WS_H, off + 1024, (u32x4){pk[4], pk[5], pk[6], pk[7]});
            } };
        gemm_loop_rt<G_ST, 0, G_SB, 4, 6, -1, Frag4>(16 * ntile, [&](int, int) {},
            [&](Frag4& f, int so, int kk, int) { lds_cptr sb = (lds_cptr)lds + so;
                f.c = ldsr128(sb + a_rd[kk]);
                f.a = cat8(vtr(sb + w_rd + kk * 2048), vtr(sb + w_rd + kk * 2048 + 512));
                f.b = cat8(vtr(sb + w_rd + 4096 + kk * 2048), vtr(sb + w_rd + 4096 + kk * 2048 + 512));
                f.d = ldsr128(sb + a_rd[kk] + 2048); },
            [&](const Frag4& f, int) {
                acc[0][0] = MFMA32(f.a, f.c, acc[0][0]); acc[0][1] = MFMA32(f.b, f.c, acc[0][1]);
                acc[1][0] = MFMA32(f.a, f.d, acc[1][0]); acc[1][1] = MFMA32(f.b, f.d, acc[1][1]); PIN(acc[0][0]); PIN(acc[0][1]); PIN(acc[1][0]); PIN(acc[1][1]); },
            [&](int s) { if (s == 15) { epilogue(cb0); acc[0][0] = (f32x16){}; acc[0][1] = (f32x16){}; acc[1][0] = (f32x16){}; acc[1][1] = (f32x16){}; SBAR(); } });
        epilogue(cb0 + 4 * (ntile - 1));
    }
    if constexpr (FUSED) {
        if (isA) { BARRIER(); }
        else { WAITV(0); BARRIER(); if (threadIdx.x == 0) flag_store(P.ws, FL_H + rb * 32 + cb0, tag); }
    } else { WAITV(0); BARRIER(); }
}

constexpr int D_ST = 4, D_AB = 8192, D_SB = 16384;
template <bool FUSED>
__device__ __forceinline__ void phase_d(const Params& P, char* lds, u64 tag) {
    const int tid = threadIdx.x, lane = tid & 63, r = lane & 31, hh = lane >> 5;
    const int w = __builtin_amdgcn_readfirstlane(tid >> 6), wk = w >> 2, wm = (w >> 1) & 1, wn = w & 1;
    const int b = blockIdx.x & 255, xcd = b & 7, bi = b >> 3, rb = xcd * 4 + (bi & 3), cb = bi >> 2;
    const int m0 = rb * 64, n0 = cb * 64;
    const int kt0 = 0;
    const unsigned lds0 = (unsigned)(uintptr_t)lds;
    constexpr int NT = 28;
    const int row = m0 + 32 * wm + r;
    u32x2 xv[2];
#pragma unroll
    for (int q = 0; q < 2; ++q) xv[q] = *(const u32x2*)(P.x + (size_t)row * 1024 + (n0 + 32 * wn + 4 * hh + 8 * (2 * wk + q)) * 2);
    const int q4 = (lane & 15) >> 2;
    const unsigned chw = (4 * wn + 2 * ((lane >> 4) & 1) + ((lane & 3) >> 1)) ^ (((q4 >> 1) & 1) << 2);
    const unsigned w_rd = (4 * hh + q4) * 128 + chw * 16 + 8 * (lane & 1);
    f32x16 acc = {};
    if (wk == 1) {
        const int p = w & 3;
        unsigned aoff[2], woff2[2];
#pragma unroll
        for (int i = 0; i < 2; ++i) { const int rw = 8 * (2 * p + i) + (lane >> 3); aoff[i] = lane * 16;
            const int ch = (lane & 7) ^ (((rw >> 1) & 1) << 2); woff2[i] = rw * 1024 + n0 * 2 + ch * 16 - 1024 * i; }
        const char* hg = uni(P.ws + WS_H + (size_t)((rb * 2 + (p >> 1)) * 112 + 2 * (p & 1)) * 1024); const char* wd = uni(P.wd);
        const unsigned ldsA = uni(lds0 + 2 * p * 1024), ldsW = uni(lds0 + D_AB + 2 * p * 1024);
        auto issue_w = [&](int it, int slot) { int kt = kt0 + it; if (kt >= NT) kt -= NT; gldsN<false, 2>(wd + kt * (64 * 1024), ldsW + slot * D_SB, woff2[0], woff2[1]); };
        auto issue_a = [&](int it, int slot) { int kt = kt0 + it; if (kt >= NT) kt -= NT; gldsN<true, 2>(hg + kt * 4096, ldsA + slot * D_SB, aoff[0], aoff[1]); };
#pragma unroll
        for (int i = 0; i < D_ST; ++i) issue_w(i, i);
        wait_flags<FUSED>(P.ws, FL_H + (rb >> 1) * 32, 28, tag);
#pragma unroll
        for (int i = 0; i < D_ST; ++i) { issue_a(i, i); issue_w(i, i); }
        loader_loop<NT, D_ST, 4>([&](int it, int slot) { issue_a(it, slot); issue_w(it, slot); });
    } else {
        wait_flags<FUSED>(P.ws, FL_H + (rb >> 1) * 32, 28, tag);
        const int arow = 32 * wm + r;
        unsigned a_rd[4];
#pragma unroll
        for (int kk = 0; kk < 4; ++kk) a_rd[kk] = wm * 4096 + kk * 1024 + lane * 16;
        struct FragD { Frag4 k0, k1; };
        auto rd = [&](Frag4& f, lds_cptr sb, int k) {
            f.a = cat8(vtr(sb + D_AB + w_rd + (2 * k) * 2048), vtr(sb + D_AB + w_rd + (2 * k) * 2048 + 1024));
            f.b = ldsr128(sb + a_rd[2 * k]);
            f.c = cat8(vtr(sb + D_AB + w_rd + (2 * k + 1) * 2048), vtr(sb + D_AB + w_rd + (2 * k + 1) * 2048 + 1024));
            f.d = ldsr128(sb + a_rd[2 * k + 1]); };
        cons_loop<NT, D_ST, D_SB, 4, 12, 0, FragD>(
            [&](FragD& f, int so, int) { lds_cptr sb = (lds_cptr)lds + so; rd(f.k0, sb, 0); rd(f.k1, sb, 1); },
            [&](const FragD& f) { acc = MFMA32(f.k0.a, f.k0.b, acc); acc = MFMA32(f.k0.c, f.k0.d, acc); acc = MFMA32(f.k1.a, f.k1.b, acc); acc = MFMA32(f.k1.c, f.k1.d, acc); PIN(acc); });
    }
    unsigned woff;
    { const int rw = 8 * w + (lane >> 3); const int ch = (lane & 7) ^ (((rw >> 1) & 1) << 2); woff = rw * 1024 + n0 * 2 + ch * 16; }
    BARRIER();
#pragma unroll
    for (int i = 0; i < 8; ++i) glds16<false>(P.wo + (size_t)i * 64 * 1024, woff, lds0 + i * 8192 + w * 1024);
    float* ex = (float*)(lds + 65536);
    if (wk == 0) {
#pragma unroll
        for (int j = 0; j < 8; ++j) ex[(w * 8 + j) * 64 + lane] = acc[8 + j];
    }
    BARRIER();
    float gate[8];
#pragma unroll
    for (int j = 0; j < 8; ++j) { const float yv = wk ? ex[((w ^ 4) * 8 + j) * 64 + lane] : acc[j]; gate[j] = yv * __builtin_amdgcn_rcpf(1.f + __builtin_amdgcn_exp2f(-1.4426950408889634f * yv)); }
    if constexpr (FUSED) {
        if (blockIdx.x == 0 && w == 7) {
            bool ok = false;
            for (unsigned spin = 0; spin < (1u << 16) && !ok; ++spin) { bool a = true;
#pragma unroll
                for (int i = 0; i < 8; ++i) a &= flag_load(P.ws, FL_START + i * 64 + lane) == tag;
                ok = __all(a); if (!ok) __builtin_amdgcn_s_sleep(8); }
            if (lane == 0) flag_store(P.ws, FL_GEN, tag);
        }
    }
    WAITV(0);
    wait_flags<FUSED>(P.ws, FL_O + (rb >> 2) * 32, 32, tag);
    u32x4 of[16];
    {
        const char* og = P.ws + WS_O + ((size_t)(rb * 2 + wm) * 32 + 16 * wk) * 1024;
#pragma unroll
        for (int i = 0; i < 16; ++i) asm volatile("s_nop 4\n\tglobal_load_dwordx4 %0, %1, %2 offset:%3 sc1" : "=v"(of[i]) : "v"(lane * 16 + (i >> 2) * 4096), "s"(uni(og)), "n"((i & 3) * 1024) : "memory");
    }
    f32x16 accS = {};
#pragma unroll
    for (int i = 0; i < 16; ++i) {
        const bf16x8 wb = cat8(vtr((lds_cptr)lds + w_rd + (16 * wk + i) * 2048), vtr((lds_cptr)lds + w_rd + (16 * wk + i) * 2048 + 1024));
        if (i == 0) asm volatile("s_waitcnt vmcnt(12)" : "+v"(of[0]), "+v"(of[1]), "+v"(of[2]), "+v"(of[3]) :: "memory");
        if (i == 4) asm volatile("s_waitcnt vmcnt(8)" : "+v"(of[4]), "+v"(of[5]), "+v"(of[6]), "+v"(of[7]) :: "memory");
        if (i == 8) asm volatile("s_waitcnt vmcnt(4)" : "+v"(of[8]), "+v"(of[9]), "+v"(of[10]), "+v"(of[11]) :: "memory");
        if (i == 12) asm volatile("s_waitcnt vmcnt(0)" : "+v"(of[12]), "+v"(of[13]), "+v"(of[14]), "+v"(of[15]) :: "memory");
        accS = MFMA32(wb, __builtin_bit_cast(bf16x8, of[i]), accS);
        if ((i & 1) == 1) SBAR();
    }
#pragma unroll
    for (int j = 0; j < 8; ++j) ex[(w * 8 + j) * 64 + lane] = accS[8 * (1 - wk) + j];
    BARRIER();
    char* orow = P.out + (size_t)row * 1024 + (n0 + 32 * wn + 4 * hh + 16 * wk) * 2;
#pragma unroll
    for (int q = 0; q < 2; ++q) {
        float res[4];
#pragma unroll
        for (int j = 0; j < 4; ++j) {
            const float sv = accS[8 * wk + 4 * q + j] + ex[((w ^ 4) * 8 + 4 * q + j) * 64 + lane];
            const float xx = (j & 1) ? bfhi(xv[q][j >> 1]) : bflo(xv[q][j >> 1]);
            res[j] = xx + sv * gate[4 * q + j];
        }
        *(u32x2*)(orow + q * 16) = (u32x2){cvtpk(res[0], res[1]), cvtpk(res[2], res[3])};
    }
}

constexpr float REF_BIAS = 88.f;
__device__ __forceinline__ void attn_slow(const char* kf, const char* vf, const bf16x8 qf, f32x16& o, int lane) {
    const int r = lane & 31;
    float m = -INFINITY; f32x16 oa = {};
    for (int t = 0; t < 64; ++t) {
        u32x4 kq, va0, va1;
        asm volatile("s_nop 4\n\tglobal_load_dwordx4 %0, %3, %4 sc1\n\tglobal_load_dwordx4 %1, %5, %6 sc1\n\tglobal_load_dwordx4 %2, %5, %6 offset:512 sc1\n\ts_waitcnt vmcnt(0)"
                     : "=&v"(kq), "=&v"(va0), "=&v"(va1) : "v"(t * 1024 + lane * 16), "s"(uni(kf)), "v"(t * 1024 + (lane >> 5) * 256 + (r & 15) * 16), "s"(uni(vf)) : "memory");
        f32x16 s = MFMA32(__builtin_bit_cast(bf16x8, kq), qf, (f32x16){});
        float tm = s[0];
#pragma unroll
        for (int i = 1; i < 16; ++i) tm = fmaxf(tm, s[i]);
        tm = swap_max(tm);
        const float mn = fmaxf(m, tm);
        const float al = __builtin_amdgcn_exp2f(m - mn);
        m = mn;
#pragma unroll
        for (int i = 0; i < 16; ++i) oa[i] *= al;
        unsigned pk[8];
#pragma unroll
        for (int i = 0; i < 8; ++i) pk[i] = cvtpk(__builtin_amdgcn_exp2f(s[2 * i] - mn), __builtin_amdgcn_exp2f(s[2 * i + 1] - mn));
        if (r >= 16) { const unsigned c = (r == 16 || r == 20) ? 0x3f803f80u : 0u; va0 = (u32x4){c, c, c, c}; va1 = va0; }
        oa = MFMA32(__builtin_bit_cast(bf16x8, va0), __builtin_bit_cast(bf16x8, ((u32x4){pk[0], pk[1], pk[2], pk[3]})), oa);
        oa = MFMA32(__builtin_bit_cast(bf16x8, va1), __builtin_bit_cast(bf16x8, ((u32x4){pk[4], pk[5], pk[6], pk[7]})), oa);
    }
    o = oa;
}

constexpr int A_SLOT = 16384, A_CONST = 65536;
template <bool FUSED>
__device__ __forceinline__ void phase_attn(const Params& P, char* lds, u64 tag) {
    const int tid = threadIdx.x, lane = tid & 63, r = lane & 31, hh = lane >> 5;
    const int w = __builtin_amdgcn_readfirstlane(tid >> 6);
    const int b = blockIdx.x & 255, xcd = b & 7, bi = b >> 3, head = xcd * 4 + (bi & 3), qb = bi >> 2;
    const unsigned lds0 = (unsigned)(uintptr_t)lds;
    const char* kf = P.ws + WS_K + (size_t)head * 65536; const char* vf = P.ws + WS_V + (size_t)head * 65536;
    const int hflag = FL_H + (8 * (xcd & 1) + (bi < 24 ? 2 * (bi & 3) : bi - 24)) * 32 + 7 * (xcd >> 1) + (bi < 24 ? bi >> 2 : 6);
    { const int row = tid & 15; const unsigned c = (row == 0 || row == 4) ? 0x3f803f80u : 0u; *(u32x4*)(lds + A_CONST + tid * 16) = (u32x4){c, c, c, c}; }
    wait_flags<FUSED>(P.ws, FL_Q + (head >> 2) * 32, 32, tag);
    if constexpr (FUSED) { if (threadIdx.x == 0) flag_store(P.ws, FL_START + blockIdx.x, tag); }
    auto issue = [&](int ci) {
        const int c = (qb + ci) & 7; const unsigned sb = lds0 + (ci & 3) * A_SLOT;
        glds16<true>(kf + c * 8192 + w * 1024, lane * 16, sb + w * 1024);
        glds16<true>(vf + c * 8192 + w * 1024, lane * 16, sb + 8192 + w * 1024);
    };
    issue(0);
    u32x4 qv;
    asm volatile("s_nop 4\n\tglobal_load_dwordx4 %0, %1, %2 sc1" : "=v"(qv) : "v"(lane * 16), "s"(uni(P.ws + WS_Q + (size_t)(head * 64 + qb * 8 + w) * 1024)) : "memory");
    issue(1); issue(2); issue(3);
    asm volatile("s_waitcnt vmcnt(6)" : "+v"(qv) :: "memory");
    BARRIER();
    if constexpr (FUSED) { if (threadIdx.x == 0) flag_store(P.ws, hflag, tag); }
    const bf16x8 qf = __builtin_bit_cast(bf16x8, qv);
    const unsigned kaddr = lane * 16;
    const unsigned vaddr = (r < 16) ? (8192 + hh * 256 + r * 16) : (A_CONST + hh * 256 + (r - 16) * 16);
    const unsigned vmul = (r < 16) ? 1u : 0u;
    f32x16 o, cneg, sA, sB;
    {
        const bf16x8 k0 = ldsr128((lds_cptr)lds + kaddr);
        const f32x16 s0 = MFMA32(k0, qf, (f32x16){});
        float tm = fmaxf(fmaxf(fmaxf(s0[0], s0[1]), fmaxf(s0[2], s0[3])), fmaxf(fmaxf(s0[4], s0[5]), fmaxf(s0[6], s0[7])));
        tm = fmaxf(tm, fmaxf(fmaxf(fmaxf(s0[8], s0[9]), fmaxf(s0[10], s0[11])), fmaxf(fmaxf(s0[12], s0[13]), fmaxf(s0[14], s0[15]))));
        tm = swap_max(tm);
        const float c = -(tm + REF_BIAS);
#pragma unroll
        for (int i = 0; i < 16; ++i) { cneg[i] = c; o[i] = 0.f; sA[i] = s0[i] + c; }
        PIN(cneg);
    }
    lds_cptr kp = (lds_cptr)lds + kaddr;
    lds_cptr vp = (lds_cptr)lds + vaddr;
    bf16x8 kfr = ldsr128(kp + 1024);
    bf16x8 v0f = ldsr128(vp), v1f;
    { u32x4 z = {0u, 0u, 0u, 0u}; v1f = __builtin_bit_cast(bf16x8, z); }
    u32x4 pk0 = {0u, 0u, 0u, 0u}, pk1 = {0u, 0u, 0u, 0u};
#define EXP2(x) __builtin_amdgcn_exp2f(x)
#define EX(dst, src) do { dst = EXP2(src); PIN(dst); } while (0)
#define CV(dst, a, b) do { dst = cvtpk(a, b); PIN(dst); } while (0)
#define BF(x) __builtin_bit_cast(bf16x8, x)
#define STEP(cur, nxt, KN, V1, V0N) do { \
        float e[16]; \
        nxt = MFMA32(kfr, qf, cneg); PIN(nxt); SBAR(); \
        PIN(cur); kfr = ldsr128(KN); SBAR(); \
        EX(e[0], cur[0]); EX(e[1], cur[1]); EX(e[2], cur[2]); EX(e[3], cur[3]); EX(e[4], cur[4]); EX(e[5], cur[5]); EX(e[6], cur[6]); SBAR(); \
        o = MFMA32(v1f, BF(pk1), o); PIN(o); SBAR(); \
        PIN(cur); PIN(e[0]); PIN(e[2]); PIN(e[4]); PIN(e[6]); v1f = ldsr128(V1); SBAR(); \
        EX(e[7], cur[7]); CV(pk0[0], e[0], e[1]); CV(pk0[1], e[2], e[3]); CV(pk0[2], e[4], e[5]); \
        EX(e[8], cur[8]); CV(pk0[3], e[6], e[7]); EX(e[9], cur[9]); EX(e[10], cur[10]); EX(e[11], cur[11]); SBAR(); \
        o = MFMA32(v0f, BF(pk0), o); PIN(o); SBAR(); \
        PIN(cur); PIN(e[8]); PIN(e[10]); v0f = ldsr128(V0N); SBAR(); \
        EX(e[12], cur[12]); EX(e[13], cur[13]); EX(e[14], cur[14]); EX(e[15], cur[15]); \
        CV(pk1[0], e[8], e[9]); CV(pk1[1], e[10], e[11]); CV(pk1[2], e[12], e[13]); CV(pk1[3], e[14], e[15]); SBAR(); \
    } while (0)
    __builtin_amdgcn_s_setprio(1);
    for (int ci = 0; ci < 8; ++ci) {
        lds_cptr kc = kp + (ci & 3) * A_SLOT, kn = kp + ((ci + 1) & 3) * A_SLOT;
        lds_cptr vc = vp + vmul * ((ci & 3) * A_SLOT), vn = vp + vmul * (((ci + 1) & 3) * A_SLOT);
        STEP(sA, sB, kc + 2 * 1024, vc + 0 * 1024 + 512, vc + 1 * 1024);
        STEP(sB, sA, kc + 3 * 1024, vc + 1 * 1024 + 512, vc + 2 * 1024);
        STEP(sA, sB, kc + 4 * 1024, vc + 2 * 1024 + 512, vc + 3 * 1024);
        STEP(sB, sA, kc + 5 * 1024, vc + 3 * 1024 + 512, vc + 4 * 1024);
        STEP(sA, sB, kc + 6 * 1024, vc + 4 * 1024 + 512, vc + 5 * 1024);
        STEP(sB, sA, kc + 7 * 1024, vc + 5 * 1024 + 512, vc + 6 * 1024);
        if (ci < 5) WAITV(2); else WAITV(0);
        asm volatile("s_barrier" ::: "memory");
        if (ci >= 1 && ci + 3 < 8) issue(ci + 3);
        STEP(sA, sB, kn + 0 * 1024, vc + 6 * 1024 + 512, vc + 7 * 1024);
        STEP(sB, sA, kn + 1 * 1024, vc + 7 * 1024 + 512, vn);
    }
    o = MFMA32(v1f, BF(pk1), o);
    bool bad = !(o[8] < 1.0e37f) || !(o[8] > 0.f);
#pragma unroll
    for (int i = 0; i < 8; ++i) bad |= !(fabsf(o[i]) < 3.0e38f);
    if (__any(bad)) attn_slow(kf, vf, qf, o, lane);
    const float rl = 1.0f / o[8];
    const u32x4 pk = {cvtpk(o[0] * rl, o[1] * rl), cvtpk(o[2] * rl, o[3] * rl), cvtpk(o[4] * rl, o[5] * rl), cvtpk(o[6] * rl, o[7] * rl)};
    st16_sc1(P.ws + WS_O, (unsigned)(((qb * 8 + w) * 32 + head) * 64 + lane) * 16, pk);
    if constexpr (FUSED) publish(P.ws, FL_O + qb * 32 + head, tag);
}


template <bool FUSED>
__device__ __forceinline__ void phase_side(const Params& P, char* lds, u64& tag) {
    const int tid = threadIdx.x, lane = tid & 63;
    const int w = __builtin_amdgcn_readfirstlane(tid >> 6);
    const int j = blockIdx.x & 255;
    const int row = 8 * j + w;
    const u32x4 xr = *(const u32x4*)(P.x + (size_t)row * 1024 + lane * 16);
    const bool has = j < 96;
    const int cgp = j % 24, sl = j / 24;
    u32x4 wv[2] = {};
    if (has) {
#pragma unroll
        for (int i = 0; i < 2; ++i) wv[i] = *(const u32x4*)(P.wqkv + (size_t)(128 * sl + 16 * w + 8 * i + (lane >> 3)) * 3072 + cgp * 128 + (lane & 7) * 16);
    }
    float v[8];
#pragma unroll
    for (int i = 0; i < 4; ++i) { v[2 * i] = bflo(xr[i]); v[2 * i + 1] = bfhi(xr[i]); }
    float sx = ((v[0] + v[1]) + (v[2] + v[3])) + ((v[4] + v[5]) + (v[6] + v[7]));
    sx = wave_sum(sx);
    TAG_READY_AFTER(sx);
    const unsigned tg = (unsigned)tag;
    const float mu = sx * (1.f / 512.f);
    float sq = 0.f;
#pragma unroll
    for (int i = 0; i < 8; ++i) { v[i] -= mu; sq = fmaf(v[i], v[i], sq); }
    sq = wave_sum(sq);
    const float rstd = __builtin_amdgcn_rsqf(sq * (1.f / 512.f) + 1e-5f);
    if (lane == 0) st16_sc1(P.ws + WS_GST, (unsigned)row * 16, (u32x4){__float_as_uint(mu), tg, __float_as_uint(rstd), tg});
    const u32x4 pk = {cvtpk(v[0] * rstd, v[1] * rstd), cvtpk(v[2] * rstd, v[3] * rstd), cvtpk(v[4] * rstd, v[5] * rstd), cvtpk(v[6] * rstd, v[7] * rstd)};
    st16_sc1(P.ws + WS_XN, (unsigned)((((lane >> 2) * 2048 + row) * 4 + ((lane & 3) ^ ((row >> 2) & 3))) * 16), pk);
    if constexpr (FUSED) publish(P.ws, FL_S + j, tag); else { WAITV(0); BARRIER(); }
    if (has) {
        float a[8];
#pragma unroll
        for (int k = 0; k < 4; ++k) { a[2 * k] = bflo(wv[0][k]) + bflo(wv[1][k]); a[2 * k + 1] = bfhi(wv[0][k]) + bfhi(wv[1][k]); }
#pragma unroll
        for (int k = 0; k < 8; ++k) a[k] = swap_add(swap16_add(dpp_add<0x128>(a[k])));
        float* red = (float*)lds;
        if (lane < 8) { *(f32x4*)&red[w * 64 + lane * 8] = (f32x4){a[0], a[1], a[2], a[3]}; *(f32x4*)&red[w * 64 + lane * 8 + 4] = (f32x4){a[4], a[5], a[6], a[7]}; }
        BARRIER();
        if (tid < 32) {
            f32x2 c = {0.f, 0.f};
#pragma unroll
            for (int i = 0; i < 8; ++i) c += *(const f32x2*)&red[i * 64 + 2 * tid];
            st16_sc1(P.ws + WS_GCS, (unsigned)(sl * 1536 + cgp * 64 + 2 * tid) * 8, (u32x4){__float_as_uint(c[0]), tg, __float_as_uint(c[1]), tg});
        }
    }
    BARRIER();
}

template <int MASK, bool FUSED>
__global__ __launch_bounds__(512, 4) void mega(Params P) {
    extern __shared__ __attribute__((aligned(16))) char lds[];
    u64 tag = 0;
    if constexpr (FUSED) {
        if ((threadIdx.x & 63) == 0)
            asm volatile("s_mov_b32 m0, %2\n\ts_nop 0\n\tglobal_load_lds_dwordx4 %0, %1 sc1" :: "v"((unsigned)(WS_FLAGS + FL_GEN * 8)), "s"(P.ws), "s"(uni((unsigned)(uintptr_t)lds + TAG_LDS + (threadIdx.x >> 6) * 16)) : "memory");
    }
    if (blockIdx.x < 256) {
        if constexpr (MASK & 1) phase_q<FUSED>(P, lds, tag);
        if constexpr (MASK & 2) phase_u<FUSED>(P, lds, tag);
        if constexpr (MASK & 4) { BARRIER(); phase_d<FUSED>(P, lds, tag); }
    } else {
        if constexpr (MASK & 1) phase_side<FUSED>(P, lds, tag);
        if constexpr (MASK & 2) phase_u<FUSED>(P, lds, tag);
        if constexpr (MASK & 8) phase_attn<FUSED>(P, lds, tag);
    }
}

extern "C" void kernel_launch(void* const* d_in, const int* in_sizes, int n_in, void* d_out, int out_size, void* d_ws, size_t ws_size, hipStream_t stream) {
    Params P;
    P.x = (const char*)d_in[0]; P.wqkv = (const char*)d_in[1]; P.wo = (const char*)d_in[2];
    P.wg = (const char*)d_in[4]; P.wu = (const char*)d_in[5]; P.wd = (const char*)d_in[6];
    P.out = (char*)d_out; P.ws = (char*)d_ws;
    (void)hipFuncSetAttribute((const void*)mega<15, true>, hipFuncAttributeMaxDynamicSharedMemorySize, LDS_BYTES);
    mega<15, true><<<512, 512, LDS_BYTES, stream>>>(P);
}
```
